```python
import torch

def _softmax(x, axis=-1):
    return torch.softmax(x, dim=axis)
def _gelu(x):
    return torch.nn.functional.gelu(x, approximate="tanh")
def _silu(x):
    return torch.nn.functional.silu(x)
def _sigmoid(x):
    return torch.sigmoid(x)
def _layernorm(x, axis=-1, eps=1e-5):
    m = x.mean(dim=axis, keepdim=True)
    v = x.var(dim=axis, keepdim=True, unbiased=False)
    return (x - m) / torch.sqrt(v + eps)
def _scatter_add(tgt, idx, src, axis=0):
    return tgt.clone().index_add_(axis, idx, src)
def _segment_sum(src, seg, n):
    out = torch.zeros((n,) + src.shape[1:], dtype=src.dtype)
    return out.index_add_(0, seg, src)
def _pad(x, pw):
    flat = tuple(v for pair in reversed(pw) for v in pair)
    return torch.nn.functional.pad(x, flat)

kernel_name = 'interleave_layers__mha_block__glu_gated_geglu__prompt'

def setup_inputs():

    _w = (lambda a: (lambda t: t.to(torch.bfloat16) if t.is_floating_point() else t)(torch.as_tensor(a)))
    def _A_setup():
        B, S, D, H = 1, 2048, 640, 32
        return {
            "x": _w((lambda *s: torch.randn(*s, dtype=torch.bfloat16) * 0.05)(B, S, D)),
            "wqkv": _w((lambda *s: torch.randn(*s, dtype=torch.bfloat16) * 0.05)(D, 3 * D)),
            "wo": _w((lambda *s: torch.randn(*s, dtype=torch.bfloat16) * 0.05)(D, D)),
        }
    _A_in = _A_setup()

    def _B_setup():
        B, S, Dm, Df = 1, 2048, 640, 1728
        return {
            "x": _w((lambda *s: torch.randn(*s, dtype=torch.bfloat16) * 0.05)(B, S, Dm)),
            "w_gate": _w((lambda *s: torch.randn(*s, dtype=torch.bfloat16) * 0.8)(Dm, Df) * (Dm ** -0.5)),
            "w_up": _w((lambda *s: torch.randn(*s, dtype=torch.bfloat16) * 0.8)(Dm, Df) * (Dm ** -0.5)),
            "w_down": _w((lambda *s: torch.randn(*s, dtype=torch.bfloat16) * 0.8)(Df, Dm) * (Df ** -0.5)),
        }
    _B_in = _B_setup()

    return {
        "A_x": _A_in["x"],
        "A_wqkv": _A_in["wqkv"],
        "A_wo": _A_in["wo"],
        "B_x": _B_in["x"],
        "B_w_gate": _B_in["w_gate"],
        "B_w_up": _B_in["w_up"],
        "B_w_down": _B_in["w_down"],
    }

def reference(A_x, A_wqkv, A_wo, B_x, B_w_gate, B_w_up, B_w_down):
    def _A_ref(x, wqkv, wo):
        B, S, D = x.shape
        H = 32
        Dh = D // H
        qkv = (lambda x, *d: x.permute(*d))((x @ wqkv).reshape(B, S, 3, H, Dh), 2, 0, 3, 1, 4)
        q, k, v = qkv[0], qkv[1], qkv[2]
        scores = torch.einsum("bhqd,bhkd->bhqk", q, k) * (Dh ** -0.5)
        p = _softmax(scores, axis=-1)
        o = torch.einsum("bhqk,bhkd->bhqd", p, v)
        return (lambda x, *d: x.permute(*d))(o, 0, 2, 1, 3).reshape(B, S, D) @ wo

    def _B_ref(x, w_gate, w_up, w_down):
        g = torch.einsum("bsd,df->bsf", x, w_gate)
        u = torch.einsum("bsd,df->bsf", x, w_up)
        h = _gelu(g) * u
        return torch.einsum("bsf,fd->bsd", h, w_down)

    x = A_x
    x = x + _A_ref(_layernorm(x, axis=-1), A_wqkv, A_wo)
    x = x + _B_ref(_layernorm(x, axis=-1), B_w_gate, B_w_up, B_w_down)
    x = x + _A_ref(_layernorm(x, axis=-1), A_wqkv, A_wo)
    x = x + _B_ref(_layernorm(x, axis=-1), B_w_gate, B_w_up, B_w_down)
    return x
```

```cpp
#include <hip/hip_runtime.h>
#include <hip/hip_bf16.h>
#include <stdint.h>

typedef unsigned short u16;
using bf16x8 = __attribute__((ext_vector_type(8))) short;
using bf16x4 = __attribute__((ext_vector_type(4))) short;
using f32x4 = __attribute__((ext_vector_type(4))) float;
using f32x16 = __attribute__((ext_vector_type(16))) float;
using u32x2 = __attribute__((ext_vector_type(2))) unsigned;
using f32x2 = __attribute__((ext_vector_type(2))) float;
using u32x4 = __attribute__((ext_vector_type(4))) unsigned;
typedef short v4i16_t __attribute__((ext_vector_type(4)));
#define LDS_AS __attribute__((address_space(3)))

constexpr int S = 2048, D = 640, H = 32, DH = 20, DF = 1728;
constexpr int NPART = 8;
constexpr int PCOLS = D / NPART;

__device__ __forceinline__ float bf2f(u16 v) { return __uint_as_float(((unsigned)v) << 16); }
__device__ __forceinline__ unsigned pack2(float lo, float hi) {
  unsigned r;
  asm("v_cvt_pk_bf16_f32 %0, %1, %2" : "=v"(r) : "v"(lo), "v"(hi));
  return r;
}
__device__ __forceinline__ float lo2f(unsigned u) { return __uint_as_float(u << 16); }
__device__ __forceinline__ float hi2f(unsigned u) { return __uint_as_float(u & 0xffff0000u); }

__device__ __forceinline__ void glds16(const void* g, LDS_AS void* l) {
  __builtin_amdgcn_global_load_lds((const __attribute__((address_space(1))) void*)g, l, 16, 0, 0);
}
__device__ __forceinline__ bf16x4 lds_tr(const LDS_AS char* p) {
  return __builtin_amdgcn_ds_read_tr16_b64_v4i16((LDS_AS v4i16_t*)p);
}
#define WAITV(n) asm volatile("s_waitcnt vmcnt(" #n ")" ::: "memory")
template <int N> __device__ __forceinline__ void waitv() {
  if constexpr (N == 0) WAITV(0);
  else if constexpr (N == 1) WAITV(1);
  else if constexpr (N == 2) WAITV(2);
  else if constexpr (N == 3) WAITV(3);
  else if constexpr (N == 4) WAITV(4);
  else if constexpr (N == 5) WAITV(5);
  else if constexpr (N == 6) WAITV(6);
  else if constexpr (N == 7) WAITV(7);
  else if constexpr (N == 8) WAITV(8);
  else if constexpr (N == 9) WAITV(9);
  else if constexpr (N == 10) WAITV(10);
  else if constexpr (N == 12) WAITV(12);
  else if constexpr (N == 14) WAITV(14);
  else if constexpr (N == 16) WAITV(16);
  else if constexpr (N == 18) WAITV(18);
  else if constexpr (N == 20) WAITV(20);
  else if constexpr (N == 24) WAITV(24);
  else static_assert(N < 0, "add waitv case");
}
__device__ __forceinline__ void barrier_raw() {
  asm volatile("s_waitcnt lgkmcnt(0)" ::: "memory");
  __builtin_amdgcn_s_barrier();
  asm volatile("" ::: "memory");
}

__global__ __launch_bounds__(256) void prep_kernel(const u16* __restrict__ x, float2* __restrict__ stats,
                                                   const u16* __restrict__ wqkv, const u16* __restrict__ wg,
                                                   const u16* __restrict__ wu, float* __restrict__ cs) {
  __shared__ float red[32][65];
  const int tid = threadIdx.x, lane = tid & 63, w = tid >> 6;
  if (blockIdx.x < 256) {
    for (int rr = 0; rr < 2; ++rr) {
      const int row = blockIdx.x * 8 + w * 2 + rr;
      const unsigned* p = (const unsigned*)(x + (size_t)row * D + lane * 10);
      float v[10];
#pragma unroll
      for (int i = 0; i < 5; ++i) { unsigned u = p[i]; v[2 * i] = lo2f(u); v[2 * i + 1] = hi2f(u); }
      float s = 0.f;
#pragma unroll
      for (int i = 0; i < 10; ++i) s += v[i];
      s += __shfl_xor(s, 1); s += __shfl_xor(s, 2); s += __shfl_xor(s, 4);
      const float mu = s * (1.f / PCOLS);
      float q = 0.f;
#pragma unroll
      for (int i = 0; i < 10; ++i) { float d = v[i] - mu; q += d * d; }
      q += __shfl_xor(q, 1); q += __shfl_xor(q, 2); q += __shfl_xor(q, 4);
      if ((lane & 7) == 0) stats[row * NPART + (lane >> 3)] = make_float2(s, q);
    }
  } else {
    const int cb = blockIdx.x - 256;
    int c0 = cb * 64;
    const u16* W; int ld; int cc;
    if (c0 < 1920) { W = wqkv; ld = 1920; cc = c0; }
    else if (c0 < 1920 + DF) { W = wg; ld = DF; cc = c0 - 1920; }
    else { W = wu; ld = DF; cc = c0 - 1920 - DF; }
    const int cg = tid & 7, rg = tid >> 3;
    float a[8] = {0.f, 0.f, 0.f, 0.f, 0.f, 0.f, 0.f, 0.f};
#pragma unroll 4
    for (int r = 0; r < 20; ++r) {
      u32x4 u = *(const u32x4*)(W + (size_t)(rg * 20 + r) * ld + cc + cg * 8);
#pragma unroll
      for (int i = 0; i < 4; ++i) { a[2 * i] += lo2f(u[i]); a[2 * i + 1] += hi2f(u[i]); }
    }
#pragma unroll
    for (int i = 0; i < 8; ++i) red[rg][cg * 8 + i] = a[i];
    __syncthreads();
    if (tid < 64) {
      float s = 0.f;
#pragma unroll
      for (int r = 0; r < 32; ++r) s += red[r][tid];
      cs[c0 + tid] = s;
    }
  }
}

enum { EPI_QKV = 0, EPI_RES = 1, EPI_GEGLU = 2 };

struct GemmArgs {
  const u16* X;
  const u16* W0;
  const u16* W1;
  const float2* stats_in;
  const float* cs0;
  const float* cs1;
  const u16* res;
  u16* out0;
  u16* out1;
  u16* out2;
  float2* stats_out;
  int ldw;
  int ncb;
};

template <int EPI, int BM, int BNW, int NW, int K, int NSTAGE, int WAVES_M, int WAVES_N, int NCOLS>
__global__ __launch_bounds__(WAVES_M * WAVES_N * 64) void gemm_kernel(GemmArgs a) {
  constexpr int NT = WAVES_M * WAVES_N * 64;
  constexpr int BK = 64, NK = K / BK;
  constexpr int XB = BM * 128;
  constexpr int WB = 64 * 256;
  constexpr int STAGE = XB + NW * WB;
  constexpr int XL = (BM * 8) / NT;
  constexpr int WL = (64 * 16) / NT;
  constexpr int LPS = XL + NW * WL;
  constexpr int MF = BM / WAVES_M / 16;
  constexpr int NFT = BNW / 16;
  constexpr int NF = NFT / WAVES_N;
  static_assert(NFT % WAVES_N == 0, "");
  static_assert((BM * 8) % NT == 0 && (64 * 16) % NT == 0, "");
  __shared__ __attribute__((aligned(16))) char smem[NSTAGE * STAGE + BM * 8];

  const int tid = threadIdx.x, lane = tid & 63;
  const int wid = __builtin_amdgcn_readfirstlane(tid >> 6);
  const int wm = wid / WAVES_N, wn = wid % WAVES_N;
  const int nrb = S / BM;
  int bid = blockIdx.x;
  {
    const int nb = gridDim.x, q = nb / 8, r = nb % 8, x = bid % 8, o = bid / 8;
    bid = (x < r ? x * (q + 1) : r * (q + 1) + (x - r) * q) + o;
  }
  const int rb = bid / a.ncb, cb = bid % a.ncb;
  (void)nrb;
  const int m0 = rb * BM, n0 = cb * BNW;

  float4 st[NPART / 2];
  if constexpr (EPI != EPI_RES) {
    if (tid < BM) {
      const float4* sp = (const float4*)(a.stats_in + (size_t)(m0 + tid) * NPART);
#pragma unroll
      for (int i = 0; i < NPART / 2; ++i) st[i] = sp[i];
    }
  }

  LDS_AS char* lds = (LDS_AS char*)smem;
  const u16* xsrc[XL];
#pragma unroll
  for (int i = 0; i < XL; ++i) {
    const int c = i * NT + tid, row = c >> 3, slot = c & 7;
    xsrc[i] = a.X + (size_t)(m0 + row) * K + ((slot ^ (row & 7)) * 8);
  }
  const u16* wsrc[NW][WL];
  bool wok[WL];
#pragma unroll
  for (int i = 0; i < WL; ++i) {
    const int c = i * NT + tid, row = c >> 4, slot = c & 15;
    const int f = (row & 3) | (((row >> 3) & 1) << 2);
    const int unit = (slot >> 1) ^ f;
    const int col = unit * 16 + (slot & 1) * 8;
    wok[i] = (col < BNW) && (n0 + col < NCOLS);
    const int colc = wok[i] ? col : 0;
    wsrc[0][i] = a.W0 + (size_t)row * a.ldw + n0 + colc;
    if constexpr (NW == 2) wsrc[1][i] = a.W1 + (size_t)row * a.ldw + n0 + colc;
  }
  auto issue = [&](int kt) {
    LDS_AS char* sb = lds + (kt % NSTAGE) * STAGE;
#pragma unroll
    for (int i = 0; i < XL; ++i) glds16(xsrc[i] + kt * BK, sb + (i * NT + wid * 64) * 16);
#pragma unroll
    for (int w = 0; w < NW; ++w)
#pragma unroll
      for (int i = 0; i < WL; ++i) {
        glds16(wsrc[w][i] + (size_t)kt * BK * a.ldw, sb + XB + w * WB + (i * NT + wid * 64) * 16);
      }
  };

  f32x4 acc[NW][NF][MF];
#pragma unroll
  for (int w = 0; w < NW; ++w)
#pragma unroll
    for (int i = 0; i < NF; ++i)
#pragma unroll
      for (int j = 0; j < MF; ++j) acc[w][i][j] = f32x4{0.f, 0.f, 0.f, 0.f};

#pragma unroll
  for (int s = 0; s < NSTAGE - 1; ++s) issue(s);

  const int g = lane >> 4, q4 = (lane & 15) >> 2, p4 = lane & 3;
  const int fl = q4 | ((g & 1) << 2);
  const int wrow_off = (8 * g + q4) * 256 + p4 * 8;
  int xoff[MF];
#pragma unroll
  for (int j = 0; j < MF; ++j) {
    const int m = wm * (MF * 16) + j * 16 + (lane & 15);
    xoff[j] = m * 128;
  }
  const int xsw = lane & 7;

  for (int kt = 0; kt < NK; ++kt) {
    if (kt + NSTAGE - 2 < NK) waitv<(NSTAGE - 2) * LPS>();
    else if constexpr (NSTAGE > 3) { if (kt + NSTAGE - 3 < NK) waitv<(NSTAGE - 3) * LPS>(); else waitv<0>(); }
    else waitv<0>();
    barrier_raw();
    if (kt + NSTAGE - 1 < NK) issue(kt + NSTAGE - 1);
    const LDS_AS char* sb = lds + (kt % NSTAGE) * STAGE;
#pragma unroll
    for (int kk = 0; kk < 2; ++kk) {
      bf16x8 xf[MF];
#pragma unroll
      for (int j = 0; j < MF; ++j)
        xf[j] = *(const LDS_AS bf16x8*)(sb + xoff[j] + (((kk * 4 + g) ^ xsw) * 16));
#pragma unroll
      for (int w = 0; w < NW; ++w) {
#pragma unroll
        for (int i = 0; i < NF; ++i) {
          const int nf = wn * NF + i;
          const LDS_AS char* wp = sb + XB + w * WB + kk * 32 * 256 + wrow_off + ((nf ^ fl) * 32);
          bf16x4 lo = lds_tr(wp), hi = lds_tr(wp + 4 * 256);
          bf16x8 wf = {lo[0], lo[1], lo[2], lo[3], hi[0], hi[1], hi[2], hi[3]};
#pragma unroll
          for (int j = 0; j < MF; ++j)
            acc[w][i][j] = __builtin_amdgcn_mfma_f32_16x16x32_bf16(wf, xf[j], acc[w][i][j], 0, 0, 0);
        }
      }
    }
  }

  LDS_AS f32x2* sst = (LDS_AS f32x2*)(lds + NSTAGE * STAGE);
  if constexpr (EPI != EPI_RES) {
    if (tid < BM) {
      float s = 0.f;
#pragma unroll
      for (int i = 0; i < NPART / 2; ++i) s += st[i].x + st[i].z;
      const float mean = s * (1.f / D);
      float m2 = 0.f;
#pragma unroll
      for (int i = 0; i < NPART / 2; ++i) {
        const float d0 = st[i].x * (1.f / PCOLS) - mean, d1 = st[i].z * (1.f / PCOLS) - mean;
        m2 += st[i].y + st[i].w + PCOLS * (d0 * d0 + d1 * d1);
      }
      const float rstd = 1.f / sqrtf(m2 * (1.f / D) + 1e-5f);
      sst[tid] = f32x2{mean, rstd};
    }
    __syncthreads();
  }

#pragma unroll
  for (int j = 0; j < MF; ++j) {
    const int ml = wm * (MF * 16) + j * 16 + (lane & 15);
    const int m = m0 + ml;
    if constexpr (EPI == EPI_QKV) {
      const float mean = sst[ml].x, rstd = sst[ml].y;
#pragma unroll
      for (int i = 0; i < NF; ++i) {
        const int n = n0 + (wn * NF + i) * 16 + 4 * g;
        const float4 c = *(const float4*)(a.cs0 + n);
        float v0 = rstd * (acc[0][i][j][0] - mean * c.x), v1 = rstd * (acc[0][i][j][1] - mean * c.y);
        float v2 = rstd * (acc[0][i][j][2] - mean * c.z), v3 = rstd * (acc[0][i][j][3] - mean * c.w);
        if (n < D) {
          const float sc = 0.22360679774997896f * 1.4426950408889634f;
          u32x2 o = {pack2(v0 * sc, v1 * sc), pack2(v2 * sc, v3 * sc)};
          *(u32x2*)(a.out0 + (size_t)m * D + n) = o;
        } else if (n < 2 * D) {
          const int c2 = n - D, h = c2 / DH, d = c2 - h * DH;
          u16* kb = a.out1 + ((size_t)(h * (S / 32) + (m >> 5)) * 1024) + (m & 31) * 8;
          u32x2 o = {pack2(v0, v1), pack2(v2, v3)};
          if (d < 16) {
            *(u32x2*)(kb + (d >> 3) * 256 + (d & 7)) = o;
          } else {
            u32x4 o4 = {o[0], o[1], 0x00003f80u, 0u};
            *(u32x4*)(kb + 512) = o4;
            *(u32x4*)(kb + 768) = u32x4{0u, 0u, 0u, 0u};
          }
        } else {
          const int c2 = n - 2 * D, h = c2 / DH, d = c2 - h * DH;
          u16* vb = a.out2 + ((size_t)h * S + m) * 32;
          u32x2 o = {pack2(v0, v1), pack2(v2, v3)};
          if (d < 16) {
            *(u32x2*)(vb + d) = o;
          } else {
            u32x4 o4 = {o[0], o[1], 0x00003f80u, 0u};
            *(u32x4*)(vb + 16) = o4;
            *(u32x4*)(vb + 24) = u32x4{0u, 0u, 0u, 0u};
          }
        }
      }
    } else if constexpr (EPI == EPI_GEGLU) {
      const float mean = sst[ml].x, rstd = sst[ml].y;
#pragma unroll
      for (int i = 0; i < NF; ++i) {
        const int n = n0 + (wn * NF + i) * 16 + 4 * g;
        if (n < NCOLS) {
          const float4 cg = *(const float4*)(a.cs0 + n);
          const float4 cu = *(const float4*)(a.cs1 + n);
          const float cgv[4] = {cg.x, cg.y, cg.z, cg.w}, cuv[4] = {cu.x, cu.y, cu.z, cu.w};
          float hv[4];
#pragma unroll
          for (int r = 0; r < 4; ++r) {
            const float gv = rstd * (acc[0][i][j][r] - mean * cgv[r]);
            const float uv = rstd * (acc[1][i][j][r] - mean * cuv[r]);
            const float z = gv * (1.f + 0.044715f * gv * gv) * (2.f * 0.7978845608028654f * 1.4426950408889634f);
            const float e = __builtin_amdgcn_exp2f(-z);
            hv[r] = gv * __builtin_amdgcn_rcpf(1.f + e) * uv;
          }
          u32x2 o = {pack2(hv[0], hv[1]), pack2(hv[2], hv[3])};
          *(u32x2*)(a.out0 + (size_t)m * NCOLS + n) = o;
        }
      }
    } else {
      float xv[NF][4];
      float s = 0.f;
#pragma unroll
      for (int i = 0; i < NF; ++i) {
        const int n = n0 + (wn * NF + i) * 16 + 4 * g;
        const u32x2 r = *(const u32x2*)(a.res + (size_t)m * D + n);
        u32x2 o = {pack2(lo2f(r[0]) + acc[0][i][j][0], hi2f(r[0]) + acc[0][i][j][1]),
                   pack2(lo2f(r[1]) + acc[0][i][j][2], hi2f(r[1]) + acc[0][i][j][3])};
        *(u32x2*)(a.out0 + (size_t)m * D + n) = o;
        xv[i][0] = lo2f(o[0]); xv[i][1] = hi2f(o[0]); xv[i][2] = lo2f(o[1]); xv[i][3] = hi2f(o[1]);
        s += (xv[i][0] + xv[i][1]) + (xv[i][2] + xv[i][3]);
      }
      s += __shfl_xor(s, 16); s += __shfl_xor(s, 32);
      const float mu = s * (1.f / PCOLS);
      float q = 0.f;
#pragma unroll
      for (int i = 0; i < NF; ++i)
#pragma unroll
        for (int r = 0; r < 4; ++r) { const float d = xv[i][r] - mu; q += d * d; }
      q += __shfl_xor(q, 16); q += __shfl_xor(q, 32);
      if (lane < 16) a.stats_out[(size_t)m * NPART + cb] = make_float2(s, q);
    }
  }
}

constexpr int ATK = 128, ANST = 3, ASTAGE = 16384;
__global__ __launch_bounds__(512) void attn_kernel(const u16* __restrict__ Q, const u16* __restrict__ Kf,
                                                   const u16* __restrict__ V, u16* __restrict__ O) {
  __shared__ __attribute__((aligned(16))) char smem[ANST * ASTAGE];
  LDS_AS char* lds = (LDS_AS char*)smem;
  const int tid = threadIdx.x, lane = tid & 63;
  const int wid = __builtin_amdgcn_readfirstlane(tid >> 6);
  const int b = blockIdx.x;
  const int h = (b & 7) + 8 * (b >> 6), qb = (b >> 3) & 7;
  const int r32 = lane & 31, hi = lane >> 5;
  const int q = qb * 256 + wid * 32 + r32;

  const u16* ksrc = Kf + (size_t)h * S * 32 + tid * 8;
  const u16* vsrc = V + (size_t)h * S * 32 + tid * 8;
  auto issue = [&](int t) {
    LDS_AS char* sb = lds + (t % ANST) * ASTAGE;
    glds16(ksrc + t * (ATK * 32), sb + wid * 1024);
    glds16(vsrc + t * (ATK * 32), sb + 8192 + wid * 1024);
  };
  bf16x8 qf0, qf1;
  {
    const u16* qp = Q + (size_t)q * D + h * DH;
    u32x2 a0 = *(const u32x2*)(qp + hi * 8), a1 = *(const u32x2*)(qp + hi * 8 + 4);
    u32x2 a2 = *(const u32x2*)(qp + 16);
    u32x4 t0 = {a0[0], a0[1], a1[0], a1[1]};
    u32x4 t1 = {hi ? 0u : a2[0], hi ? 0u : a2[1], 0u, 0u};
    qf0 = __builtin_bit_cast(bf16x8, t0);
    qf1 = __builtin_bit_cast(bf16x8, t1);
  }
  issue(0); issue(1);

  const int koff = lane * 16;
  const int G = lane >> 4;
  const int voff = 8192 + (4 * hi + ((lane & 15) >> 2)) * 64 + (G & 1) * 32 + (lane & 3) * 8;
  f32x16 o = {};
  constexpr int NTILE = S / ATK;
  for (int t = 0; t < NTILE; ++t) {
    if (t + 1 < NTILE) WAITV(2); else WAITV(0);
    barrier_raw();
    if (t + 2 < NTILE) issue(t + 2);
    const LDS_AS char* sb = lds + (t % ANST) * ASTAGE;
    if (t == 0) {
      bf16x8 kf0 = *(const LDS_AS bf16x8*)(sb + koff), kf1 = *(const LDS_AS bf16x8*)(sb + koff + 1024);
      f32x16 s = __builtin_amdgcn_mfma_f32_32x32x16_bf16(kf0, qf0, f32x16{}, 0, 0, 0);
      s = __builtin_amdgcn_mfma_f32_32x32x16_bf16(kf1, qf1, s, 0, 0, 0);
      float m = s[0];
#pragma unroll
      for (int r = 1; r < 16; ++r) m = fmaxf(m, s[r]);
      m = fmaxf(m, __shfl_xor(m, 32));
      const unsigned mb = pack2(-m, 0.f) & 0xffffu;
      u32x4 t1 = __builtin_bit_cast(u32x4, qf1);
      if (!hi) t1[2] = mb;
      qf1 = __builtin_bit_cast(bf16x8, t1);
    }
#pragma unroll
    for (int j = 0; j < 4; ++j) {
      bf16x8 kf0 = *(const LDS_AS bf16x8*)(sb + j * 2048 + koff);
      bf16x8 kf1 = *(const LDS_AS bf16x8*)(sb + j * 2048 + koff + 1024);
      f32x16 s = __builtin_amdgcn_mfma_f32_32x32x16_bf16(kf0, qf0, f32x16{}, 0, 0, 0);
      s = __builtin_amdgcn_mfma_f32_32x32x16_bf16(kf1, qf1, s, 0, 0, 0);
      const LDS_AS char* vp = sb + voff + j * 2048;
      bf16x4 v00 = lds_tr(vp), v01 = lds_tr(vp + 512), v10 = lds_tr(vp + 1024), v11 = lds_tr(vp + 1536);
      bf16x8 vf0 = {v00[0], v00[1], v00[2], v00[3], v01[0], v01[1], v01[2], v01[3]};
      bf16x8 vf1 = {v10[0], v10[1], v10[2], v10[3], v11[0], v11[1], v11[2], v11[3]};
      float p[16];
#pragma unroll
      for (int r = 0; r < 16; ++r) p[r] = __builtin_amdgcn_exp2f(s[r]);
      u32x4 pb0 = {pack2(p[0], p[1]), pack2(p[2], p[3]), pack2(p[4], p[5]), pack2(p[6], p[7])};
      u32x4 pb1 = {pack2(p[8], p[9]), pack2(p[10], p[11]), pack2(p[12], p[13]), pack2(p[14], p[15])};
      o = __builtin_amdgcn_mfma_f32_32x32x16_bf16(vf0, __builtin_bit_cast(bf16x8, pb0), o, 0, 0, 0);
      o = __builtin_amdgcn_mfma_f32_32x32x16_bf16(vf1, __builtin_bit_cast(bf16x8, pb1), o, 0, 0, 0);
    }
  }
  float l = __shfl(o[8], r32 + 32);
  bool bad = !(l < 1.0e18f);
#pragma unroll
  for (int r = 0; r < 12; ++r) bad = bad || !(fabsf(o[r]) < 3.0e38f);
  if (__any(bad)) {
    float qv[DH];
    {
      const u16* qp = Q + (size_t)q * D + h * DH;
#pragma unroll
      for (int d = 0; d < DH; ++d) qv[d] = bf2f(qp[d]);
    }
    float m = -INFINITY, ls = 0.f, ov[DH];
#pragma unroll
    for (int d = 0; d < DH; ++d) ov[d] = 0.f;
    for (int key = hi; key < S; key += 2) {
      const u16* kb = Kf + ((size_t)(h * (S / 32) + (key >> 5)) * 1024) + (key & 31) * 8;
      float sc = 0.f;
#pragma unroll
      for (int d = 0; d < DH; ++d) sc += qv[d] * bf2f(kb[(d >> 3) * 256 + (d & 7)]);
      const float mn = fmaxf(m, sc);
      const float al = __builtin_amdgcn_exp2f(m - mn), pe = __builtin_amdgcn_exp2f(sc - mn);
      const u16* vb = V + ((size_t)h * S + key) * 32;
      ls = ls * al + pe;
#pragma unroll
      for (int d = 0; d < DH; ++d) ov[d] = ov[d] * al + pe * bf2f(vb[d]);
      m = mn;
    }
    const float m2 = __shfl_xor(m, 32), mn = fmaxf(m, m2);
    const float al = __builtin_amdgcn_exp2f(m - mn);
    ls *= al; ls += __shfl_xor(ls, 32);
    const float rl = 1.f / ls;
    if (!hi) {
#pragma unroll
      for (int d = 0; d < DH; ++d) ov[d] *= al;
    } else {
#pragma unroll
      for (int d = 0; d < DH; ++d) ov[d] *= al;
    }
#pragma unroll
    for (int d = 0; d < DH; ++d) ov[d] = (ov[d] + __shfl_xor(ov[d], 32)) * rl;
    if (!hi) {
      u16* op = O + (size_t)q * D + h * DH;
#pragma unroll
      for (int d = 0; d < DH; d += 4) {
        u32x2 w = {pack2(ov[d], ov[d + 1]), pack2(ov[d + 2], ov[d + 3])};
        *(u32x2*)(op + d) = w;
      }
    }
    return;
  }
  const float rl = 1.f / l;
  u16* op = O + (size_t)q * D + h * DH + 4 * hi;
#pragma unroll
  for (int gq = 0; gq < 3; ++gq) {
    if (gq < 2 || !hi) {
      u32x2 w = {pack2(o[4 * gq] * rl, o[4 * gq + 1] * rl), pack2(o[4 * gq + 2] * rl, o[4 * gq + 3] * rl)};
      *(u32x2*)(op + 8 * gq) = w;
    }
  }
}

extern "C" void kernel_launch(void* const* d_in, const int* in_sizes, int n_in, void* d_out, int out_size,
                              void* d_ws, size_t ws_size, hipStream_t stream) {
  const u16* x0 = (const u16*)d_in[0];
  const u16* wqkv = (const u16*)d_in[1];
  const u16* wo = (const u16*)d_in[2];
  const u16* wg = (const u16*)d_in[4];
  const u16* wu = (const u16*)d_in[5];
  const u16* wd = (const u16*)d_in[6];
  char* ws = (char*)d_ws;
  size_t off = 0;
  auto alloc = [&](size_t bytes) { char* p = ws + off; off += (bytes + 255) & ~(size_t)255; return p; };
  float2* stats = (float2*)alloc((size_t)S * NPART * 8);
  float* cs = (float*)alloc(5376 * 4);
  u16* Qb = (u16*)alloc((size_t)S * D * 2);
  u16* Kf = (u16*)alloc((size_t)H * S * 32 * 2);
  u16* Vb = (u16*)alloc((size_t)H * S * 32 * 2);
  u16* Ob = (u16*)alloc((size_t)S * D * 2);
  u16* xa = (u16*)alloc((size_t)S * D * 2);
  u16* xb = (u16*)alloc((size_t)S * D * 2);
  u16* Hb = (u16*)alloc((size_t)S * DF * 2);

  prep_kernel<<<256 + 84, 256, 0, stream>>>(x0, stats, wqkv, wg, wu, cs);

  const u16* xin = x0;
  for (int layer = 0; layer < 2; ++layer) {
    u16* x1 = xa;
    u16* x2 = layer == 0 ? xb : (u16*)d_out;
    {
      GemmArgs a{}; a.X = xin; a.W0 = wqkv; a.W1 = nullptr; a.stats_in = stats; a.cs0 = cs; a.cs1 = nullptr; a.res = nullptr;
      a.out0 = Qb; a.out1 = Kf; a.out2 = Vb; a.stats_out = nullptr; a.ldw = 1920; a.ncb = 15;
      gemm_kernel<EPI_QKV, 128, 128, 1, 640, 4, 2, 2, 1920><<<16 * 15, 256, 0, stream>>>(a);
    }
    attn_kernel<<<256, 512, 0, stream>>>(Qb, Kf, Vb, Ob);
    {
      GemmArgs a{}; a.X = Ob; a.W0 = wo; a.W1 = nullptr; a.stats_in = nullptr; a.cs0 = nullptr; a.cs1 = nullptr; a.res = xin;
      a.out0 = x1; a.out1 = nullptr; a.out2 = nullptr; a.stats_out = stats; a.ldw = 640; a.ncb = 8;
      gemm_kernel<EPI_RES, 64, 80, 1, 640, 4, 4, 1, 640><<<32 * 8, 256, 0, stream>>>(a);
    }
    {
      GemmArgs a{}; a.X = x1; a.W0 = wg; a.W1 = wu; a.stats_in = stats; a.cs0 = cs + 1920; a.cs1 = cs + 1920 + DF; a.res = nullptr;
      a.out0 = Hb; a.out1 = nullptr; a.out2 = nullptr; a.stats_out = nullptr; a.ldw = DF; a.ncb = 14;
      gemm_kernel<EPI_GEGLU, 128, 128, 2, 640, 3, 2, 2, DF><<<16 * 14, 256, 0, stream>>>(a);
    }
    {
      GemmArgs a{}; a.X = Hb; a.W0 = wd; a.W1 = nullptr; a.stats_in = nullptr; a.cs0 = nullptr; a.cs1 = nullptr; a.res = x1;
      a.out0 = x2; a.out1 = nullptr; a.out2 = nullptr; a.stats_out = stats; a.ldw = 640; a.ncb = 8;
      gemm_kernel<EPI_RES, 64, 80, 1, DF, 4, 4, 1, 640><<<32 * 8, 256, 0, stream>>>(a);
    }
    xin = x2;
  }
}
```

```cpp
#include <hip/hip_runtime.h>
#include <hip/hip_bf16.h>
#include <stdint.h>

typedef unsigned short u16;
using bf16x8 = __attribute__((ext_vector_type(8))) short;
using bf16x4 = __attribute__((ext_vector_type(4))) short;
using f32x4 = __attribute__((ext_vector_type(4))) float;
using f32x16 = __attribute__((ext_vector_type(16))) float;
using u32x2 = __attribute__((ext_vector_type(2))) unsigned;
using f32x2 = __attribute__((ext_vector_type(2))) float;
using u32x4 = __attribute__((ext_vector_type(4))) unsigned;
typedef short v4i16_t __attribute__((ext_vector_type(4)));
#define LDS_AS __attribute__((address_space(3)))

constexpr int S = 2048, D = 640, H = 32, DH = 20, DF = 1728;
constexpr int NPART = 8;
constexpr int PCOLS = D / NPART;

__device__ __forceinline__ float bf2f(u16 v) { return __uint_as_float(((unsigned)v) << 16); }
__device__ __forceinline__ unsigned pack2(float lo, float hi) {
  unsigned r;
  asm("v_cvt_pk_bf16_f32 %0, %1, %2" : "=v"(r) : "v"(lo), "v"(hi));
  return r;
}
__device__ __forceinline__ float lo2f(unsigned u) { return __uint_as_float(u << 16); }
__device__ __forceinline__ float hi2f(unsigned u) { return __uint_as_float(u & 0xffff0000u); }

__device__ __forceinline__ void glds16(const void* g, LDS_AS void* l) {
  const unsigned lb = __builtin_amdgcn_readfirstlane((unsigned)(uintptr_t)l);
  unsigned sv;
  asm volatile("s_mov_b32 %0, m0\n\ts_mov_b32 m0, %2\n\ts_nop 0\n\tglobal_load_lds_dwordx4 %1, off\n\ts_mov_b32 m0, %0"
               : "=&s"(sv) : "v"(g), "s"(lb) : "memory");
}
__device__ __forceinline__ bf16x4 lds_tr(const LDS_AS char* p) {
  return __builtin_amdgcn_ds_read_tr16_b64_v4i16((LDS_AS v4i16_t*)p);
}
#define WAITV(n) asm volatile("s_waitcnt vmcnt(" #n ")" ::: "memory")
template <int N> __device__ __forceinline__ void waitv() {
  if constexpr (N == 0) WAITV(0);
  else if constexpr (N == 1) WAITV(1);
  else if constexpr (N == 2) WAITV(2);
  else if constexpr (N == 3) WAITV(3);
  else if constexpr (N == 4) WAITV(4);
  else if constexpr (N == 5) WAITV(5);
  else if constexpr (N == 6) WAITV(6);
  else if constexpr (N == 7) WAITV(7);
  else if constexpr (N == 8) WAITV(8);
  else if constexpr (N == 9) WAITV(9);
  else if constexpr (N == 10) WAITV(10);
  else if constexpr (N == 12) WAITV(12);
  else if constexpr (N == 14) WAITV(14);
  else if constexpr (N == 16) WAITV(16);
  else if constexpr (N == 18) WAITV(18);
  else if constexpr (N == 20) WAITV(20);
  else if constexpr (N == 24) WAITV(24);
  else static_assert(N < 0, "add waitv case");
}
__device__ __forceinline__ void barrier_raw() {
  asm volatile("s_waitcnt lgkmcnt(0)" ::: "memory");
  __builtin_amdgcn_s_barrier();
  asm volatile("" ::: "memory");
}

__global__ __launch_bounds__(256) void prep_kernel(const u16* __restrict__ x, float2* __restrict__ stats,
                                                   const u16* __restrict__ wqkv, const u16* __restrict__ wg,
                                                   const u16* __restrict__ wu, float* __restrict__ cs) {
  __shared__ float red[32][65];
  const int tid = threadIdx.x, lane = tid & 63, w = tid >> 6;
  if (blockIdx.x < 256) {
    for (int rr = 0; rr < 2; ++rr) {
      const int row = blockIdx.x * 8 + w * 2 + rr;
      const unsigned* p = (const unsigned*)(x + (size_t)row * D + lane * 10);
      float v[10];
#pragma unroll
      for (int i = 0; i < 5; ++i) { unsigned u = p[i]; v[2 * i] = lo2f(u); v[2 * i + 1] = hi2f(u); }
      float s = 0.f;
#pragma unroll
      for (int i = 0; i < 10; ++i) s += v[i];
      s += __shfl_xor(s, 1); s += __shfl_xor(s, 2); s += __shfl_xor(s, 4);
      const float mu = s * (1.f / PCOLS);
      float q = 0.f;
#pragma unroll
      for (int i = 0; i < 10; ++i) { float d = v[i] - mu; q += d * d; }
      q += __shfl_xor(q, 1); q += __shfl_xor(q, 2); q += __shfl_xor(q, 4);
      if ((lane & 7) == 0) stats[row * NPART + (lane >> 3)] = make_float2(s, q);
    }
  } else {
    const int cb = blockIdx.x - 256;
    int c0 = cb * 64;
    const u16* W; int ld; int cc;
    if (c0 < 1920) { W = wqkv; ld = 1920; cc = c0; }
    else if (c0 < 1920 + DF) { W = wg; ld = DF; cc = c0 - 1920; }
    else { W = wu; ld = DF; cc = c0 - 1920 - DF; }
    const int cg = tid & 7, rg = tid >> 3;
    float a[8] = {0.f, 0.f, 0.f, 0.f, 0.f, 0.f, 0.f, 0.f};
#pragma unroll 4
    for (int r = 0; r < 20; ++r) {
      u32x4 u = *(const u32x4*)(W + (size_t)(rg * 20 + r) * ld + cc + cg * 8);
#pragma unroll
      for (int i = 0; i < 4; ++i) { a[2 * i] += lo2f(u[i]); a[2 * i + 1] += hi2f(u[i]); }
    }
#pragma unroll
    for (int i = 0; i < 8; ++i) red[rg][cg * 8 + i] = a[i];
    __syncthreads();
    if (tid < 64) {
      float s = 0.f;
#pragma unroll
      for (int r = 0; r < 32; ++r) s += red[r][tid];
      cs[c0 + tid] = s;
    }
  }
}

enum { EPI_QKV = 0, EPI_RES = 1, EPI_GEGLU = 2 };

struct GemmArgs {
  const u16* X;
  const u16* W0;
  const u16* W1;
  const float2* stats_in;
  const float* cs0;
  const float* cs1;
  const u16* res;
  u16* out0;
  u16* out1;
  u16* out2;
  float2* stats_out;
  int ldw;
  int ncb;
};

constexpr int NLW = 4;
template <int EPI, int BM, int BNW, int NW, int K, int NSTAGE, int WAVES_M, int WAVES_N, int NCOLS>
__global__ __launch_bounds__((WAVES_M * WAVES_N + NLW) * 64) void gemm_kernel(GemmArgs a) {
  constexpr int NCW = WAVES_M * WAVES_N;
  constexpr int NT = NLW * 64;
  constexpr int BK = 64, NK = K / BK;
  constexpr int XB = BM * 128;
  constexpr int WB = 64 * 256;
  constexpr int STAGE = XB + NW * WB;
  constexpr int XL = (BM * 8) / NT;
  constexpr int WL = (64 * 16) / NT;
  constexpr int LPS = XL + NW * WL;
  constexpr int MF = BM / WAVES_M / 16;
  constexpr int NFT = BNW / 16;
  constexpr int NF = NFT / WAVES_N;
  static_assert(NFT % WAVES_N == 0, "");
  static_assert((BM * 8) % NT == 0 && (64 * 16) % NT == 0, "");
  __shared__ __attribute__((aligned(16))) char smem[NSTAGE * STAGE + BM * 8];

  const int tid = threadIdx.x, lane = tid & 63;
  const int wid = __builtin_amdgcn_readfirstlane(tid >> 6);
  int bid = blockIdx.x;
  {
    const int nb = gridDim.x, q = nb / 8, r = nb % 8, x = bid % 8, o = bid / 8;
    bid = (x < r ? x * (q + 1) : r * (q + 1) + (x - r) * q) + o;
  }
  const int rb = bid / a.ncb, cb = bid % a.ncb;
  const int m0 = rb * BM, n0 = cb * BNW;
  LDS_AS char* lds = (LDS_AS char*)smem;
  LDS_AS f32x2* sst = (LDS_AS f32x2*)(lds + NSTAGE * STAGE);

  if (wid >= NCW) {
    const int lt = tid - NCW * 64, lw = wid - NCW;
    const u16* xsrc[XL];
#pragma unroll
    for (int i = 0; i < XL; ++i) {
      const int c = i * NT + lt, row = c >> 3, slot = c & 7;
      xsrc[i] = a.X + (size_t)(m0 + row) * K + ((slot ^ (row & 7)) * 8);
    }
    const u16* wsrc[NW][WL];
    bool wok[WL];
#pragma unroll
    for (int i = 0; i < WL; ++i) {
      const int c = i * NT + lt, row = c >> 4, slot = c & 15;
      const int f = (row & 3) | (((row >> 3) & 1) << 2);
      const int unit = (slot >> 1) ^ f;
      const int col = unit * 16 + (slot & 1) * 8;
      wok[i] = (BNW == 128 && NCOLS % 128 == 0) || ((col < BNW) && (n0 + col < NCOLS));
      wsrc[0][i] = a.W0 + (size_t)row * a.ldw + n0 + col;
      if constexpr (NW == 2) wsrc[1][i] = a.W1 + (size_t)row * a.ldw + n0 + col;
    }
    auto issue = [&](int kt) {
      LDS_AS char* sb = lds + (kt % NSTAGE) * STAGE;
#pragma unroll
      for (int i = 0; i < XL; ++i) glds16(xsrc[i] + kt * BK, sb + (i * NT + lw * 64) * 16);
#pragma unroll
      for (int w = 0; w < NW; ++w)
#pragma unroll
        for (int i = 0; i < WL; ++i) {
          if (wok[i]) glds16(wsrc[w][i] + (size_t)kt * BK * a.ldw, sb + XB + w * WB + (i * NT + lw * 64) * 16);
        }
    };
#pragma unroll
    for (int s = 0; s < NSTAGE - 1; ++s) issue(s);
    for (int kt = 0; kt < NK; ++kt) {
      if (kt + NSTAGE - 2 < NK) waitv<(NSTAGE - 2) * LPS>();
      else if constexpr (NSTAGE > 3) { if (kt + NSTAGE - 3 < NK) waitv<(NSTAGE - 3) * LPS>(); else waitv<0>(); }
      else waitv<0>();
      barrier_raw();
      if (kt + NSTAGE - 1 < NK) issue(kt + NSTAGE - 1);
    }
    if constexpr (EPI != EPI_RES) {
      if (lt < BM) {
        float4 st[NPART / 2];
        const float4* sp = (const float4*)(a.stats_in + (size_t)(m0 + lt) * NPART);
#pragma unroll
        for (int i = 0; i < NPART / 2; ++i) st[i] = sp[i];
        float s = 0.f;
#pragma unroll
        for (int i = 0; i < NPART / 2; ++i) s += st[i].x + st[i].z;
        const float mean = s * (1.f / D);
        float m2 = 0.f;
#pragma unroll
        for (int i = 0; i < NPART / 2; ++i) {
          const float d0 = st[i].x * (1.f / PCOLS) - mean, d1 = st[i].z * (1.f / PCOLS) - mean;
          m2 += st[i].y + st[i].w + PCOLS * (d0 * d0 + d1 * d1);
        }
        const float rstd = 1.f / sqrtf(m2 * (1.f / D) + 1e-5f);
        sst[lt] = f32x2{mean, rstd};
      }
    }
    if constexpr (EPI != EPI_RES) barrier_raw();
    return;
  }

  const int wm = wid / WAVES_N, wn = wid % WAVES_N;
  float4 csr[NW][NF];
  u32x2 resr[NF][MF];
  if constexpr (EPI != EPI_RES) {
#pragma unroll
    for (int i = 0; i < NF; ++i) {
      int n = n0 + (wn * NF + i) * 16 + 4 * (lane >> 4);
      n = n < NCOLS ? n : 0;
      csr[0][i] = *(const float4*)(a.cs0 + n);
      if constexpr (NW == 2) csr[1][i] = *(const float4*)(a.cs1 + n);
    }
  } else {
#pragma unroll
    for (int j = 0; j < MF; ++j)
#pragma unroll
      for (int i = 0; i < NF; ++i) {
        const int m = m0 + wm * (MF * 16) + j * 16 + (lane & 15);
        const int n = n0 + (wn * NF + i) * 16 + 4 * (lane >> 4);
        resr[i][j] = *(const u32x2*)(a.res + (size_t)m * D + n);
      }
  }

  f32x4 acc[NW][NF][MF];
#pragma unroll
  for (int w = 0; w < NW; ++w)
#pragma unroll
    for (int i = 0; i < NF; ++i)
#pragma unroll
      for (int j = 0; j < MF; ++j) acc[w][i][j] = f32x4{0.f, 0.f, 0.f, 0.f};

  const int g = lane >> 4, q4 = (lane & 15) >> 2, p4 = lane & 3;
  const int fl = q4 | ((g & 1) << 2);
  const int wrow_off = (8 * g + q4) * 256 + p4 * 8;
  int xoff[MF];
#pragma unroll
  for (int j = 0; j < MF; ++j) {
    const int m = wm * (MF * 16) + j * 16 + (lane & 15);
    xoff[j] = m * 128;
  }
  const int xsw = lane & 7;

  for (int kt = 0; kt < NK; ++kt) {
    barrier_raw();
    const LDS_AS char* sb = lds + (kt % NSTAGE) * STAGE;
#pragma unroll
    for (int kk = 0; kk < 2; ++kk) {
      bf16x8 xf[MF];
#pragma unroll
      for (int j = 0; j < MF; ++j)
        xf[j] = *(const LDS_AS bf16x8*)(sb + xoff[j] + (((kk * 4 + g) ^ xsw) * 16));
#pragma unroll
      for (int w = 0; w < NW; ++w) {
#pragma unroll
        for (int i = 0; i < NF; ++i) {
          const int nf = wn * NF + i;
          const LDS_AS char* wp = sb + XB + w * WB + kk * 32 * 256 + wrow_off + ((nf ^ fl) * 32);
          bf16x4 lo = lds_tr(wp), hi = lds_tr(wp + 4 * 256);
          bf16x8 wf = {lo[0], lo[1], lo[2], lo[3], hi[0], hi[1], hi[2], hi[3]};
#pragma unroll
          for (int j = 0; j < MF; ++j)
            acc[w][i][j] = __builtin_amdgcn_mfma_f32_16x16x32_bf16(wf, xf[j], acc[w][i][j], 0, 0, 0);
        }
      }
    }
  }
  __builtin_amdgcn_s_waitcnt(0x0F70);
  if constexpr (EPI != EPI_RES) barrier_raw();

#pragma unroll
  for (int j = 0; j < MF; ++j) {
    const int ml = wm * (MF * 16) + j * 16 + (lane & 15);
    const int m = m0 + ml;
    if constexpr (EPI == EPI_QKV) {
      const float mean = sst[ml].x, rstd = sst[ml].y;
#pragma unroll
      for (int i = 0; i < NF; ++i) {
        const int n = n0 + (wn * NF + i) * 16 + 4 * g;
        const float4 c = csr[0][i];
        float v0 = rstd * (acc[0][i][j][0] - mean * c.x), v1 = rstd * (acc[0][i][j][1] - mean * c.y);
        float v2 = rstd * (acc[0][i][j][2] - mean * c.z), v3 = rstd * (acc[0][i][j][3] - mean * c.w);
        if (n < D) {
          const float sc = 0.22360679774997896f * 1.4426950408889634f;
          u32x2 o = {pack2(v0 * sc, v1 * sc), pack2(v2 * sc, v3 * sc)};
          *(u32x2*)(a.out0 + (size_t)m * D + n) = o;
        } else if (n < 2 * D) {
          const int c2 = n - D, h = c2 / DH, d = c2 - h * DH;
          u16* kb = a.out1 + ((size_t)(h * (S / 32) + (m >> 5)) * 1024) + (m & 31) * 8;
          u32x2 o = {pack2(v0, v1), pack2(v2, v3)};
          if (d < 16) {
            *(u32x2*)(kb + (d >> 3) * 256 + (d & 7)) = o;
          } else {
            u32x4 o4 = {o[0], o[1], 0x00003f80u, 0u};
            *(u32x4*)(kb + 512) = o4;
            *(u32x4*)(kb + 768) = u32x4{0u, 0u, 0u, 0u};
          }
        } else {
          const int c2 = n - 2 * D, h = c2 / DH, d = c2 - h * DH;
          u16* vb = a.out2 + ((size_t)h * S + m) * 32;
          u32x2 o = {pack2(v0, v1), pack2(v2, v3)};
          if (d < 16) {
            *(u32x2*)(vb + d) = o;
          } else {
            u32x4 o4 = {o[0], o[1], 0x00003f80u, 0u};
            *(u32x4*)(vb + 16) = o4;
            *(u32x4*)(vb + 24) = u32x4{0u, 0u, 0u, 0u};
          }
        }
      }
    } else if constexpr (EPI == EPI_GEGLU) {
      const float mean = sst[ml].x, rstd = sst[ml].y;
#pragma unroll
      for (int i = 0; i < NF; ++i) {
        const int n = n0 + (wn * NF + i) * 16 + 4 * g;
        if (n < NCOLS) {
          const float4 cg = csr[0][i];
          const float4 cu = csr[NW - 1][i];
          const float cgv[4] = {cg.x, cg.y, cg.z, cg.w}, cuv[4] = {cu.x, cu.y, cu.z, cu.w};
          float hv[4];
#pragma unroll
          for (int r = 0; r < 4; ++r) {
            const float gv = rstd * (acc[0][i][j][r] - mean * cgv[r]);
            const float uv = rstd * (acc[1][i][j][r] - mean * cuv[r]);
            const float z = gv * (1.f + 0.044715f * gv * gv) * (2.f * 0.7978845608028654f * 1.4426950408889634f);
            const float e = __builtin_amdgcn_exp2f(-z);
            hv[r] = gv * __builtin_amdgcn_rcpf(1.f + e) * uv;
          }
          u32x2 o = {pack2(hv[0], hv[1]), pack2(hv[2], hv[3])};
          *(u32x2*)(a.out0 + (size_t)m * NCOLS + n) = o;
        }
      }
    } else {
      float xv[NF][4];
      float s = 0.f;
#pragma unroll
      for (int i = 0; i < NF; ++i) {
        const int n = n0 + (wn * NF + i) * 16 + 4 * g;
        const u32x2 r = resr[i][j];
        u32x2 o = {pack2(lo2f(r[0]) + acc[0][i][j][0], hi2f(r[0]) + acc[0][i][j][1]),
                   pack2(lo2f(r[1]) + acc[0][i][j][2], hi2f(r[1]) + acc[0][i][j][3])};
        *(u32x2*)(a.out0 + (size_t)m * D + n) = o;
        xv[i][0] = lo2f(o[0]); xv[i][1] = hi2f(o[0]); xv[i][2] = lo2f(o[1]); xv[i][3] = hi2f(o[1]);
        s += (xv[i][0] + xv[i][1]) + (xv[i][2] + xv[i][3]);
      }
      s += __shfl_xor(s, 16); s += __shfl_xor(s, 32);
      const float mu = s * (1.f / PCOLS);
      float q = 0.f;
#pragma unroll
      for (int i = 0; i < NF; ++i)
#pragma unroll
        for (int r = 0; r < 4; ++r) { const float d = xv[i][r] - mu; q += d * d; }
      q += __shfl_xor(q, 16); q += __shfl_xor(q, 32);
      if (lane < 16) a.stats_out[(size_t)m * NPART + cb] = make_float2(s, q);
    }
  }
}

constexpr int ATK = 256, ANST = 3, ASTAGE = 32768;
__global__ __launch_bounds__(1024) void attn_kernel(const u16* __restrict__ Q, const u16* __restrict__ Kf,
                                                    const u16* __restrict__ V, u16* __restrict__ O) {
  __shared__ __attribute__((aligned(16))) char smem[ANST * ASTAGE];
  LDS_AS char* lds = (LDS_AS char*)smem;
  const int tid = threadIdx.x, lane = tid & 63;
  const int wid = __builtin_amdgcn_readfirstlane(tid >> 6);
  const int qg = wid & 7, kh = wid >> 3;
  const int b = blockIdx.x;
  const int h = (b & 7) + 8 * (b >> 6), qb = (b >> 3) & 7;
  const int r32 = lane & 31, hi = lane >> 5;
  const int q = qb * 256 + qg * 32 + r32;

  const u16* ksrc = Kf + (size_t)h * S * 32 + tid * 8;
  const u16* vsrc = V + (size_t)h * S * 32 + tid * 8;
  auto issue = [&](int t) {
    LDS_AS char* sb = lds + (t % ANST) * ASTAGE;
    glds16(ksrc + t * (ATK * 32), sb + wid * 1024);
    glds16(vsrc + t * (ATK * 32), sb + 16384 + wid * 1024);
  };
  issue(0); issue(1);
  bf16x8 qf0, qf1;
  {
    const u16* qp = Q + (size_t)q * D + h * DH;
    u32x2 a0 = *(const u32x2*)(qp + hi * 8), a1 = *(const u32x2*)(qp + hi * 8 + 4);
    u32x2 a2 = *(const u32x2*)(qp + 16);
    u32x4 t0 = {a0[0], a0[1], a1[0], a1[1]};
    u32x4 t1 = {hi ? 0u : a2[0], hi ? 0u : a2[1], 0u, 0u};
    qf0 = __builtin_bit_cast(bf16x8, t0);
    qf1 = __builtin_bit_cast(bf16x8, t1);
  }

  const int koff = lane * 16 + kh * 8192;
  const int G = lane >> 4;
  const int voff = 16384 + kh * 8192 + (4 * hi + ((lane & 15) >> 2)) * 64 + (G & 1) * 32 + (lane & 3) * 8;
  f32x16 o = {};
  constexpr int NTILE = S / ATK;
  for (int t = 0; t < NTILE; ++t) {
    if (t + 1 < NTILE) WAITV(2); else WAITV(0);
    barrier_raw();
    if (t + 2 < NTILE) issue(t + 2);
    const LDS_AS char* sb = lds + (t % ANST) * ASTAGE;
    if (t == 0) {
      bf16x8 kf0 = *(const LDS_AS bf16x8*)(sb + lane * 16), kf1 = *(const LDS_AS bf16x8*)(sb + lane * 16 + 1024);
      f32x16 s = __builtin_amdgcn_mfma_f32_32x32x16_bf16(kf0, qf0, f32x16{}, 0, 0, 0);
      s = __builtin_amdgcn_mfma_f32_32x32x16_bf16(kf1, qf1, s, 0, 0, 0);
      float m = s[0];
#pragma unroll
      for (int r = 1; r < 16; ++r) m = fmaxf(m, s[r]);
      m = fmaxf(m, __shfl_xor(m, 32));
      const unsigned mb = pack2(-m, 0.f) & 0xffffu;
      u32x4 t1 = __builtin_bit_cast(u32x4, qf1);
      if (!hi) t1[2] = mb;
      qf1 = __builtin_bit_cast(bf16x8, t1);
    }
#pragma unroll
    for (int j = 0; j < 4; ++j) {
      bf16x8 kf0 = *(const LDS_AS bf16x8*)(sb + j * 2048 + koff);
      bf16x8 kf1 = *(const LDS_AS bf16x8*)(sb + j * 2048 + koff + 1024);
      f32x16 s = __builtin_amdgcn_mfma_f32_32x32x16_bf16(kf0, qf0, f32x16{}, 0, 0, 0);
      s = __builtin_amdgcn_mfma_f32_32x32x16_bf16(kf1, qf1, s, 0, 0, 0);
      const LDS_AS char* vp = sb + voff + j * 2048;
      bf16x4 v00 = lds_tr(vp), v01 = lds_tr(vp + 512), v10 = lds_tr(vp + 1024), v11 = lds_tr(vp + 1536);
      bf16x8 vf0 = {v00[0], v00[1], v00[2], v00[3], v01[0], v01[1], v01[2], v01[3]};
      bf16x8 vf1 = {v10[0], v10[1], v10[2], v10[3], v11[0], v11[1], v11[2], v11[3]};
      float p[16];
#pragma unroll
      for (int r = 0; r < 16; ++r) p[r] = __builtin_amdgcn_exp2f(s[r]);
      u32x4 pb0 = {pack2(p[0], p[1]), pack2(p[2], p[3]), pack2(p[4], p[5]), pack2(p[6], p[7])};
      u32x4 pb1 = {pack2(p[8], p[9]), pack2(p[10], p[11]), pack2(p[12], p[13]), pack2(p[14], p[15])};
      o = __builtin_amdgcn_mfma_f32_32x32x16_bf16(vf0, __builtin_bit_cast(bf16x8, pb0), o, 0, 0, 0);
      o = __builtin_amdgcn_mfma_f32_32x32x16_bf16(vf1, __builtin_bit_cast(bf16x8, pb1), o, 0, 0, 0);
    }
  }
  barrier_raw();
  {
    LDS_AS f32x4* ex = (LDS_AS f32x4*)(lds + qg * 4096);
    if (kh == 1) {
#pragma unroll
      for (int r = 0; r < 3; ++r) ex[r * 64 + lane] = f32x4{o[4 * r], o[4 * r + 1], o[4 * r + 2], o[4 * r + 3]};
    }
    barrier_raw();
    if (kh == 1) return;
#pragma unroll
    for (int r = 0; r < 3; ++r) {
      const f32x4 e = ex[r * 64 + lane];
      o[4 * r] += e[0]; o[4 * r + 1] += e[1]; o[4 * r + 2] += e[2]; o[4 * r + 3] += e[3];
    }
  }
  float l = __shfl(o[8], r32 + 32);
  bool bad = !(l < 1.0e18f);
#pragma unroll
  for (int r = 0; r < 12; ++r) bad = bad || !(fabsf(o[r]) < 3.0e38f);
  if (__any(bad)) {
    float qv[DH];
    {
      const u16* qp = Q + (size_t)q * D + h * DH;
#pragma unroll
      for (int d = 0; d < DH; ++d) qv[d] = bf2f(qp[d]);
    }
    float m = -INFINITY, ls = 0.f, ov[DH];
#pragma unroll
    for (int d = 0; d < DH; ++d) ov[d] = 0.f;
    for (int key = hi; key < S; key += 2) {
      const u16* kb = Kf + ((size_t)(h * (S / 32) + (key >> 5)) * 1024) + (key & 31) * 8;
      float sc = 0.f;
#pragma unroll
      for (int d = 0; d < DH; ++d) sc += qv[d] * bf2f(kb[(d >> 3) * 256 + (d & 7)]);
      const float mn = fmaxf(m, sc);
      const float al = __builtin_amdgcn_exp2f(m - mn), pe = __builtin_amdgcn_exp2f(sc - mn);
      const u16* vb = V + ((size_t)h * S + key) * 32;
      ls = ls * al + pe;
#pragma unroll
      for (int d = 0; d < DH; ++d) ov[d] = ov[d] * al + pe * bf2f(vb[d]);
      m = mn;
    }
    const float m2 = __shfl_xor(m, 32), mn = fmaxf(m, m2);
    const float al = __builtin_amdgcn_exp2f(m - mn);
    ls *= al; ls += __shfl_xor(ls, 32);
    const float rl = 1.f / ls;
    if (!hi) {
#pragma unroll
      for (int d = 0; d < DH; ++d) ov[d] *= al;
    } else {
#pragma unroll
      for (int d = 0; d < DH; ++d) ov[d] *= al;
    }
#pragma unroll
    for (int d = 0; d < DH; ++d) ov[d] = (ov[d] + __shfl_xor(ov[d], 32)) * rl;
    if (!hi) {
      u16* op = O + (size_t)q * D + h * DH;
#pragma unroll
      for (int d = 0; d < DH; d += 4) {
        u32x2 w = {pack2(ov[d], ov[d + 1]), pack2(ov[d + 2], ov[d + 3])};
        *(u32x2*)(op + d) = w;
      }
    }
    return;
  }
  const float rl = 1.f / l;
  u16* op = O + (size_t)q * D + h * DH + 4 * hi;
#pragma unroll
  for (int gq = 0; gq < 3; ++gq) {
    if (gq < 2 || !hi) {
      u32x2 w = {pack2(o[4 * gq] * rl, o[4 * gq + 1] * rl), pack2(o[4 * gq + 2] * rl, o[4 * gq + 3] * rl)};
      *(u32x2*)(op + 8 * gq) = w;
    }
  }
}

extern "C" void kernel_launch(void* const* d_in, const int* in_sizes, int n_in, void* d_out, int out_size,
                              void* d_ws, size_t ws_size, hipStream_t stream) {
  const u16* x0 = (const u16*)d_in[0];
  const u16* wqkv = (const u16*)d_in[1];
  const u16* wo = (const u16*)d_in[2];
  const u16* wg = (const u16*)d_in[4];
  const u16* wu = (const u16*)d_in[5];
  const u16* wd = (const u16*)d_in[6];
  char* ws = (char*)d_ws;
  size_t off = 0;
  auto alloc = [&](size_t bytes) { char* p = ws + off; off += (bytes + 255) & ~(size_t)255; return p; };
  float2* stats = (float2*)alloc((size_t)S * NPART * 8);
  float* cs = (float*)alloc(5376 * 4);
  u16* Qb = (u16*)alloc((size_t)S * D * 2);
  u16* Kf = (u16*)alloc((size_t)H * S * 32 * 2);
  u16* Vb = (u16*)alloc((size_t)H * S * 32 * 2);
  u16* Ob = (u16*)alloc((size_t)S * D * 2);
  u16* xa = (u16*)alloc((size_t)S * D * 2);
  u16* xb = (u16*)alloc((size_t)S * D * 2);
  u16* Hb = (u16*)alloc((size_t)S * DF * 2);

  prep_kernel<<<256 + 84, 256, 0, stream>>>(x0, stats, wqkv, wg, wu, cs);

  const u16* xin = x0;
  for (int layer = 0; layer < 2; ++layer) {
    u16* x1 = xa;
    u16* x2 = layer == 0 ? xb : (u16*)d_out;
    {
      GemmArgs a{}; a.X = xin; a.W0 = wqkv; a.W1 = nullptr; a.stats_in = stats; a.cs0 = cs; a.cs1 = nullptr; a.res = nullptr;
      a.out0 = Qb; a.out1 = Kf; a.out2 = Vb; a.stats_out = nullptr; a.ldw = 1920; a.ncb = 15;
      gemm_kernel<EPI_QKV, 128, 128, 1, 640, 4, 2, 2, 1920><<<16 * 15, 512, 0, stream>>>(a);
    }
    attn_kernel<<<256, 1024, 0, stream>>>(Qb, Kf, Vb, Ob);
    {
      GemmArgs a{}; a.X = Ob; a.W0 = wo; a.W1 = nullptr; a.stats_in = nullptr; a.cs0 = nullptr; a.cs1 = nullptr; a.res = xin;
      a.out0 = x1; a.out1 = nullptr; a.out2 = nullptr; a.stats_out = stats; a.ldw = 640; a.ncb = 8;
      gemm_kernel<EPI_RES, 64, 80, 1, 640, 4, 4, 1, 640><<<32 * 8, 512, 0, stream>>>(a);
    }
    {
      GemmArgs a{}; a.X = x1; a.W0 = wg; a.W1 = wu; a.stats_in = stats; a.cs0 = cs + 1920; a.cs1 = cs + 1920 + DF; a.res = nullptr;
      a.out0 = Hb; a.out1 = nullptr; a.out2 = nullptr; a.stats_out = nullptr; a.ldw = DF; a.ncb = 14;
      gemm_kernel<EPI_GEGLU, 128, 128, 2, 640, 3, 2, 2, DF><<<16 * 14, 512, 0, stream>>>(a);
    }
    {
      GemmArgs a{}; a.X = Hb; a.W0 = wd; a.W1 = nullptr; a.stats_in = nullptr; a.cs0 = nullptr; a.cs1 = nullptr; a.res = x1;
      a.out0 = x2; a.out1 = nullptr; a.out2 = nullptr; a.stats_out = stats; a.ldw = 640; a.ncb = 8;
      gemm_kernel<EPI_RES, 64, 80, 1, DF, 4, 4, 1, 640><<<32 * 8, 512, 0, stream>>>(a);
    }
    xin = x2;
  }
}
```

```cpp
#include <hip/hip_runtime.h>
#include <hip/hip_bf16.h>
#include <stdint.h>

typedef unsigned short u16;
using bf16x8 = __attribute__((ext_vector_type(8))) short;
using bf16x4 = __attribute__((ext_vector_type(4))) short;
using f32x4 = __attribute__((ext_vector_type(4))) float;
using f32x16 = __attribute__((ext_vector_type(16))) float;
using u32x2 = __attribute__((ext_vector_type(2))) unsigned;
using f32x2 = __attribute__((ext_vector_type(2))) float;
using u32x4 = __attribute__((ext_vector_type(4))) unsigned;
typedef short v4i16_t __attribute__((ext_vector_type(4)));
#define LDS_AS __attribute__((address_space(3)))

constexpr int S = 2048, D = 640, H = 32, DH = 20, DF = 1728;
constexpr int NPART = 8;
constexpr int PCOLS = D / NPART;
constexpr int HP = 24;

#define TSDECL
#define TS(i)
#define TSFLUSH(kid)

__device__ __forceinline__ float bf2f(u16 v) { return __uint_as_float(((unsigned)v) << 16); }
__device__ __forceinline__ unsigned pack2(float lo, float hi) {
  unsigned r;
  asm("v_cvt_pk_bf16_f32 %0, %1, %2" : "=v"(r) : "v"(lo), "v"(hi));
  return r;
}
__device__ __forceinline__ float lo2f(unsigned u) { return __uint_as_float(u << 16); }
__device__ __forceinline__ float hi2f(unsigned u) { return __uint_as_float(u & 0xffff0000u); }

__device__ __forceinline__ void glds16(const void* g, unsigned lb) {
  unsigned sv;
  asm volatile("s_mov_b32 %0, m0\n\ts_mov_b32 m0, %2\n\ts_nop 0\n\tglobal_load_lds_dwordx4 %1, off\n\ts_mov_b32 m0, %0"
               : "=&s"(sv) : "v"(g), "s"(lb) : "memory");
}
__device__ __forceinline__ bf16x4 lds_tr(const LDS_AS char* p) {
  return __builtin_amdgcn_ds_read_tr16_b64_v4i16((LDS_AS v4i16_t*)p);
}
template <int N> __device__ __forceinline__ void waitv() {
  static_assert(N >= 0 && N < 64, "");
  asm volatile("s_waitcnt vmcnt(%0)" :: "n"(N) : "memory");
}
__device__ __forceinline__ void barrier_raw() {
  asm volatile("s_waitcnt lgkmcnt(0)" ::: "memory");
  __builtin_amdgcn_s_barrier();
  asm volatile("" ::: "memory");
}

__global__ __launch_bounds__(256) void prep_kernel(const u16* __restrict__ x, float2* __restrict__ stats,
                                                   const u16* __restrict__ wqkv, const u16* __restrict__ wg,
                                                   const u16* __restrict__ wu, float* __restrict__ cs) {
  __shared__ float red[32][65];
  const int tid = threadIdx.x, lane = tid & 63, w = tid >> 6;
  if (blockIdx.x < 256) {
    for (int rr = 0; rr < 2; ++rr) {
      const int row = blockIdx.x * 8 + w * 2 + rr;
      const unsigned* p = (const unsigned*)(x + (size_t)row * D + lane * 10);
      float v[10];
#pragma unroll
      for (int i = 0; i < 5; ++i) { unsigned u = p[i]; v[2 * i] = lo2f(u); v[2 * i + 1] = hi2f(u); }
      float s = 0.f;
#pragma unroll
      for (int i = 0; i < 10; ++i) s += v[i];
      s += __shfl_xor(s, 1); s += __shfl_xor(s, 2); s += __shfl_xor(s, 4);
      const float mu = s * (1.f / PCOLS);
      float q = 0.f;
#pragma unroll
      for (int i = 0; i < 10; ++i) { float d = v[i] - mu; q += d * d; }
      q += __shfl_xor(q, 1); q += __shfl_xor(q, 2); q += __shfl_xor(q, 4);
      if ((lane & 7) == 0) stats[row * NPART + (lane >> 3)] = make_float2(s, q);
    }
  } else {
    const int cb = blockIdx.x - 256;
    int c0 = cb * 64;
    const u16* W; int ld; int cc;
    if (c0 < 1920) { W = wqkv; ld = 1920; cc = c0; }
    else if (c0 < 1920 + DF) { W = wg; ld = DF; cc = c0 - 1920; }
    else { W = wu; ld = DF; cc = c0 - 1920 - DF; }
    const int cg = tid & 7, rg = tid >> 3;
    float a[8] = {0.f, 0.f, 0.f, 0.f, 0.f, 0.f, 0.f, 0.f};
#pragma unroll 4
    for (int r = 0; r < 20; ++r) {
      u32x4 u = *(const u32x4*)(W + (size_t)(rg * 20 + r) * ld + cc + cg * 8);
#pragma unroll
      for (int i = 0; i < 4; ++i) { a[2 * i] += lo2f(u[i]); a[2 * i + 1] += hi2f(u[i]); }
    }
#pragma unroll
    for (int i = 0; i < 8; ++i) red[rg][cg * 8 + i] = a[i];
    __syncthreads();
    if (tid < 64) {
      float s = 0.f;
#pragma unroll
      for (int r = 0; r < 32; ++r) s += red[r][tid];
      cs[c0 + tid] = s;
    }
  }
}

enum { EPI_QKV = 0, EPI_RES = 1, EPI_GEGLU = 2 };

struct GemmArgs {
  const u16* X;
  const u16* W0;
  const u16* W1;
  const float2* stats_in;
  const float* cs0;
  const float* cs1;
  const u16* res;
  u16* out0;
  float2* stats_out;
  int kid;
  int pad_;
};

constexpr int NLW = 4, NCW = 4;
template <int EPI, int BM, int BNP, int BNS, int NW, int K, int BK, int NS, int WN, int LDW, int NCB, int CPX>
__global__ __launch_bounds__(512) void gemm_kernel(GemmArgs a) {
  TSDECL; TS(0);
  constexpr int NK = K / BK, UPS = BK / 32, NU = K / 32;
  constexpr int XROWB = BK * 2, XCPR = XROWB / 16;
  constexpr int XB = BM * XROWB;
  constexpr int WP = BNP * 2, CPR = WP / 16;
  constexpr int WB = BK * WP;
  constexpr int STAGE = XB + NW * WB;
  constexpr int XI = XB / 1024, WI = WB / 1024, T = XI + NW * WI;
  constexpr int LPA = (T + NLW - 1) / NLW, NBIG = T - (LPA - 1) * NLW;
  constexpr int WM = NCW / WN;
  constexpr int MF = BM / WM / 16;
  constexpr int NFT = BNP / 16;
  constexpr int NF = (NW == 2) ? NFT : NFT / WN;
  constexpr int NCOLS = LDW;
  constexpr int RPX = 32 / CPX, NCG = NCB / CPX;
  static_assert(XB % 1024 == 0 && WB % 1024 == 0, "");
  static_assert(NS * STAGE + BM * 8 <= 163840, "LDS");
  __shared__ __attribute__((aligned(16))) char smem[NS * STAGE + BM * 8];

  const int tid = threadIdx.x, lane = tid & 63;
  const int wid = __builtin_amdgcn_readfirstlane(tid >> 6);
  const int xcd = blockIdx.x & 7, ob = blockIdx.x >> 3;
  const int rb = (xcd / NCG) * RPX + ob / CPX, cb = (xcd % NCG) * CPX + ob % CPX;
  const int m0 = rb * BM, n0 = cb * BNS;
  const int c0 = (BNP > BNS && EPI == EPI_GEGLU) ? (n0 + BNP <= NCOLS ? n0 : NCOLS - BNP) : n0;
  const int csh = n0 - c0;
  LDS_AS char* lds = (LDS_AS char*)smem;
  const unsigned lds0 = (unsigned)(uintptr_t)lds;
  LDS_AS f32x2* sst = (LDS_AS f32x2*)(lds + NS * STAGE);

  if (wid >= NCW) {
    const int lt = tid - NCW * 64, lw = wid - NCW;
    const char* src[LPA];
    unsigned strd[LPA];
    bool ok[LPA];
#pragma unroll
    for (int it = 0; it < LPA; ++it) {
      const int t = it * NLW + lw;
      if (t < XI) {
        const int c = t * 64 + lane, row = c / XCPR, slot = c % XCPR;
        const int ch = (BK == 64) ? (slot ^ (row & 7)) : (slot ^ ((row & 8) ? 3 : 0));
        src[it] = (const char*)(a.X + (size_t)(m0 + row) * K + ch * 8);
        strd[it] = BK * 2;
        ok[it] = true;
      } else {
        const int tw = t - XI, w = (NW == 2 && tw >= WI) ? 1 : 0;
        const int c = (tw - w * WI) * 64 + lane, rho = c / CPR, slot = c % CPR;
        const int r5 = rho & 31, k = (rho & ~31) + 8 * ((r5 >> 2) & 3) + 4 * (r5 >> 4) + (r5 & 3);
        const int col = (WP == 256) ? ((((slot >> 1) ^ (rho & 7)) * 16) + (slot & 1) * 8) : slot * 8;
        ok[it] = (EPI == EPI_QKV) ? (col < BNS) : true;
        const u16* W = w ? a.W1 : a.W0;
        src[it] = (const char*)(W + (size_t)k * LDW + c0 + col);
        strd[it] = BK * LDW * 2;
      }
    }
    auto issue = [&](int kt) {
      const unsigned sb = lds0 + (kt % NS) * STAGE + lw * 1024;
#pragma unroll
      for (int it = 0; it < LPA; ++it) {
        if (it < LPA - 1 || lw < NBIG) {
          if (ok[it]) glds16(src[it], sb + it * (NLW * 1024));
          src[it] += strd[it];
        }
      }
    };
#pragma unroll
    for (int s = 0; s < NS; ++s) if (s < NK) issue(s);
    if (lw < NBIG) waitv<(NS - 1) * LPA>(); else waitv<(NS - 1) * (LPA - 1)>();
    barrier_raw();
    TS(1);
    for (int j = 0; j < NK - 1; ++j) {
      if (j + NS <= NK) { if (lw < NBIG) waitv<(NS - 2) * LPA>(); else waitv<(NS - 2) * (LPA - 1)>(); }
      else {
        const int rem = NK - 2 - j;
        if (rem >= 5) { if (lw < NBIG) waitv<5 * LPA>(); else waitv<5 * (LPA - 1)>(); }
        else if (rem == 4) { if (lw < NBIG) waitv<4 * LPA>(); else waitv<4 * (LPA - 1)>(); }
        else if (rem == 3) { if (lw < NBIG) waitv<3 * LPA>(); else waitv<3 * (LPA - 1)>(); }
        else if (rem == 2) { if (lw < NBIG) waitv<2 * LPA>(); else waitv<2 * (LPA - 1)>(); }
        else if (rem == 1) { if (lw < NBIG) waitv<LPA>(); else waitv<LPA - 1>(); }
        else waitv<0>();
      }
      barrier_raw();
      if (j + NS < NK) issue(j + NS);
    }
    TS(2);
    if constexpr (EPI != EPI_RES) {
      if (lt < BM) {
        float4 st[NPART / 2];
        const float4* sp = (const float4*)(a.stats_in + (size_t)(m0 + lt) * NPART);
#pragma unroll
        for (int i = 0; i < NPART / 2; ++i) st[i] = sp[i];
        float s = 0.f;
#pragma unroll
        for (int i = 0; i < NPART / 2; ++i) s += st[i].x + st[i].z;
        const float mean = s * (1.f / D);
        float m2 = 0.f;
#pragma unroll
        for (int i = 0; i < NPART / 2; ++i) {
          const float d0 = st[i].x * (1.f / PCOLS) - mean, d1 = st[i].z * (1.f / PCOLS) - mean;
          m2 += st[i].y + st[i].w + PCOLS * (d0 * d0 + d1 * d1);
        }
        const float rstd = 1.f / sqrtf(m2 * (1.f / D) + 1e-5f);
        sst[lt] = f32x2{mean, rstd};
      }
      barrier_raw();
      barrier_raw();
    }
  } else {
    const int wm = wid / WN, wn = wid % WN;
    const int wsel = (NW == 2) ? wn : 0;
    const int nfb = (NW == 2) ? 0 : wn * NF;
    const int g = lane >> 4;
    float4 csr[NF];
    u32x2 resr[NF][MF];
    if constexpr (EPI != EPI_RES) {
      const float* cs = wsel ? a.cs1 : a.cs0;
#pragma unroll
      for (int i = 0; i < NF; ++i) {
        int n = c0 + (nfb + i) * 16 + 4 * g;
        n = n < NCOLS ? n : 0;
        csr[i] = *(const float4*)(cs + n);
      }
    } else {
#pragma unroll
      for (int j = 0; j < MF; ++j)
#pragma unroll
        for (int i = 0; i < NF; ++i) {
          const int m = m0 + wm * (MF * 16) + j * 16 + (lane & 15);
          const int n = n0 + (nfb + i) * 16 + 4 * g;
          resr[i][j] = *(const u32x2*)(a.res + (size_t)m * D + n);
        }
    }
    f32x4 acc[NF][MF];
#pragma unroll
    for (int i = 0; i < NF; ++i)
#pragma unroll
      for (int j = 0; j < MF; ++j) acc[i][j] = f32x4{0.f, 0.f, 0.f, 0.f};

    const int xrow = wm * (MF * 16) + (lane & 15);
    int xo[UPS];
#pragma unroll
    for (int kk = 0; kk < UPS; ++kk)
      xo[kk] = xrow * XROWB + ((BK == 64) ? (((kk * 4 + g) ^ (lane & 7)) * 16) : ((g ^ ((lane & 8) ? 3 : 0)) * 16));
    int wo[(WP == 256) ? NF : 1];
    if constexpr (WP == 256) {
#pragma unroll
      for (int i = 0; i < NF; ++i) wo[i] = XB + (lane >> 2) * WP + (((nfb + i) ^ ((lane >> 2) & 7)) * 32) + (lane & 3) * 8;
    } else {
      wo[0] = XB + wsel * WB + (lane >> 2) * WP + nfb * 32 + (lane & 3) * 8;
    }
    bf16x8 xf[2][MF], wf[2][NF];
    auto rdfrags = [&](int u, int slot) {
      const LDS_AS char* sb = lds + ((u / UPS) % NS) * STAGE;
      const int kk = u % UPS;
#pragma unroll
      for (int j = 0; j < MF; ++j) xf[slot][j] = *(const LDS_AS bf16x8*)(sb + xo[kk] + j * 16 * XROWB);
#pragma unroll
      for (int i = 0; i < NF; ++i) {
        const LDS_AS char* wp = sb + kk * 32 * WP + ((WP == 256) ? wo[i] : wo[0] + i * 32);
        bf16x4 lo = lds_tr(wp), hi = lds_tr(wp + 16 * WP);
        wf[slot][i] = bf16x8{lo[0], lo[1], lo[2], lo[3], hi[0], hi[1], hi[2], hi[3]};
      }
    };
    auto mfmas = [&](int slot) {
#pragma unroll
      for (int i = 0; i < NF; ++i)
#pragma unroll
        for (int j = 0; j < MF; ++j)
          acc[i][j] = __builtin_amdgcn_mfma_f32_16x16x32_bf16(wf[slot][i], xf[slot][j], acc[i][j], 0, 0, 0);
    };
    barrier_raw();
    TS(1);
    rdfrags(0, 0);
    for (int u = 0; u < NU; u += 2) {
      if (UPS == 1) barrier_raw();
      rdfrags(u + 1, 1);
      __builtin_amdgcn_sched_barrier(0);
      mfmas(0);
      __builtin_amdgcn_sched_barrier(0);
      if (u + 2 < NU) {
        barrier_raw();
        rdfrags(u + 2, 0);
      }
      __builtin_amdgcn_sched_barrier(0);
      mfmas(1);
      __builtin_amdgcn_sched_barrier(0);
    }
    TS(2);
    __builtin_amdgcn_s_waitcnt(0x0F70);

    if constexpr (EPI == EPI_QKV) {
      barrier_raw();
      TS(3);
#pragma unroll
      for (int j = 0; j < MF; ++j) {
        const int ml = wm * (MF * 16) + j * 16 + (lane & 15);
        const f32x2 st = sst[ml];
#pragma unroll
        for (int i = 0; i < NF; ++i) {
          const int nl = (nfb + i) * 16 + 4 * g;
          const float sc = (n0 + nl < D) ? st.y * (0.22360679774997896f * 1.4426950408889634f) : st.y;
          const float4 c = csr[i];
          u32x2 o = {pack2(sc * (acc[i][j][0] - st.x * c.x), sc * (acc[i][j][1] - st.x * c.y)),
                     pack2(sc * (acc[i][j][2] - st.x * c.z), sc * (acc[i][j][3] - st.x * c.w))};
          *(LDS_AS u32x2*)(lds + ml * 264 + nl * 2) = o;
        }
      }
      barrier_raw();
    } else if constexpr (EPI == EPI_GEGLU) {
      barrier_raw();
      TS(3);
#pragma unroll
      for (int j = 0; j < MF; ++j) {
        const int ml = wm * (MF * 16) + j * 16 + (lane & 15);
        const f32x2 st = sst[ml];
#pragma unroll
        for (int i = 0; i < NF; ++i) {
          const int nl = i * 16 + 4 * g;
          const float4 c = csr[i];
          f32x4 o = {st.y * (acc[i][j][0] - st.x * c.x), st.y * (acc[i][j][1] - st.x * c.y),
                     st.y * (acc[i][j][2] - st.x * c.z), st.y * (acc[i][j][3] - st.x * c.w)};
          *(LDS_AS f32x4*)(lds + wsel * (BM * 464) + ml * 464 + nl * 4) = o;
        }
      }
      barrier_raw();
    } else {
      TS(3);
#pragma unroll
      for (int j = 0; j < MF; ++j) {
        const int m = m0 + wm * (MF * 16) + j * 16 + (lane & 15);
        float xv[NF][4];
        float s = 0.f;
#pragma unroll
        for (int i = 0; i < NF; ++i) {
          const int n = n0 + (nfb + i) * 16 + 4 * g;
          const u32x2 r = resr[i][j];
          u32x2 o = {pack2(lo2f(r[0]) + acc[i][j][0], hi2f(r[0]) + acc[i][j][1]),
                     pack2(lo2f(r[1]) + acc[i][j][2], hi2f(r[1]) + acc[i][j][3])};
          *(u32x2*)(a.out0 + (size_t)m * D + n) = o;
          xv[i][0] = lo2f(o[0]); xv[i][1] = hi2f(o[0]); xv[i][2] = lo2f(o[1]); xv[i][3] = hi2f(o[1]);
          s += (xv[i][0] + xv[i][1]) + (xv[i][2] + xv[i][3]);
        }
        s += __shfl_xor(s, 16); s += __shfl_xor(s, 32);
        const float mu = s * (1.f / PCOLS);
        float q = 0.f;
#pragma unroll
        for (int i = 0; i < NF; ++i)
#pragma unroll
          for (int r = 0; r < 4; ++r) { const float d = xv[i][r] - mu; q += d * d; }
        q += __shfl_xor(q, 16); q += __shfl_xor(q, 32);
        if (lane < 16) a.stats_out[(size_t)m * NPART + cb] = make_float2(s, q);
      }
    }
  }
  TS(4);
  if constexpr (EPI == EPI_QKV) {
#pragma unroll
    for (int p = 0; p < 5; ++p) {
      const int slot = p * 512 + tid;
      if (slot < 6 * 384) {
        const int hh = slot / 384, r = slot - hh * 384, m = r / 3, j = r - 3 * m;
        const int gh = cb * 6 + hh;
        const LDS_AS char* sp = lds + m * 264 + hh * 40 + j * 16;
        const u32x2 v0 = *(const LDS_AS u32x2*)sp;
        u32x2 v1 = *(const LDS_AS u32x2*)(sp + 8);
        if (j == 2) v1 = u32x2{gh < H ? 0u : 0x00003f80u, 0u};
        *(u32x4*)((char*)a.out0 + ((size_t)gh * S + m0) * (HP * 2) + r * 16) = u32x4{v0[0], v0[1], v1[0], v1[1]};
      }
    }
  } else if constexpr (EPI == EPI_GEGLU) {
#pragma unroll
    for (int p = 0; p < 4; ++p) {
      const int slot = p * 512 + tid;
      if (slot < BM * 14) {
        const int m = slot / 14, s8 = slot - m * 14;
        const LDS_AS char* gp = lds + m * 464 + (csh + s8 * 8) * 4;
        const f32x4 g0 = *(const LDS_AS f32x4*)gp, g1 = *(const LDS_AS f32x4*)(gp + 16);
        const f32x4 u0 = *(const LDS_AS f32x4*)(gp + BM * 464), u1 = *(const LDS_AS f32x4*)(gp + BM * 464 + 16);
        float hv[8];
#pragma unroll
        for (int r = 0; r < 8; ++r) {
          const float gv = r < 4 ? g0[r & 3] : g1[r & 3], uv = r < 4 ? u0[r & 3] : u1[r & 3];
          const float z = gv * (1.f + 0.044715f * gv * gv) * (2.f * 0.7978845608028654f * 1.4426950408889634f);
          const float e = __builtin_amdgcn_exp2f(-z);
          hv[r] = gv * __builtin_amdgcn_rcpf(1.f + e) * uv;
        }
        u16* op = a.out0 + (size_t)(m0 + m) * NCOLS + n0 + s8 * 8;
        const u32x2 o0 = {pack2(hv[0], hv[1]), pack2(hv[2], hv[3])};
        const u32x2 o1 = {pack2(hv[4], hv[5]), pack2(hv[6], hv[7])};
        if (s8 < 13) {
          *(u32x2*)op = o0; *(u32x2*)(op + 4) = o1;
        } else {
          *(u32x2*)op = o0;
        }
      }
    }
  }
  TS(5);
  TSFLUSH(a.kid);
}

constexpr int ATK = 256, ANS = 4, AKB = ATK * HP * 2, ASTAGE = 2 * AKB;
struct AFr { bf16x8 k0, k1; };
__global__ __launch_bounds__(512) void attn_kernel(const u16* __restrict__ QKV, u16* __restrict__ O, int kid) {
  TSDECL; TS(0);
  __shared__ __attribute__((aligned(16))) char smem[ANS * ASTAGE + 64 + 12288];
  LDS_AS char* lds = (LDS_AS char*)smem;
  const unsigned lds0 = (unsigned)(uintptr_t)lds;
  const int tid = threadIdx.x, lane = tid & 63;
  const int wid = __builtin_amdgcn_readfirstlane(tid >> 6);
  const int qg = wid & 3, kh = wid >> 2;
  const int b = blockIdx.x;
  const int h = (b & 7) + 8 * (b >> 6), qb = (b >> 3) & 7;
  const int r32 = lane & 31, hi = lane >> 5;
  const int q0 = qb * 256 + qg * 64 + r32;
  const u16* Qh = QKV + (size_t)h * S * HP;
  const u16* Kh = QKV + (size_t)(H + h) * S * HP;
  const u16* Vh = QKV + (size_t)(2 * H + h) * S * HP;

  {
    const char* qs = (const char*)(Qh + (size_t)qb * 256 * HP) + lane * 16;
    glds16(qs + wid * 1024, lds0 + ANS * ASTAGE + 64 + wid * 1024);
    if (wid < 4) glds16(qs + (8 + wid) * 1024, lds0 + ANS * ASTAGE + 64 + (8 + wid) * 1024);
  }
  const char* dsrc[3];
#pragma unroll
  for (int i = 0; i < 3; ++i) {
    const int p = wid + 8 * i;
    dsrc[i] = (p < 12 ? (const char*)Kh + p * 1024 : (const char*)Vh + (p - 12) * 1024) + lane * 16;
  }
  auto issue = [&](int t) {
    const unsigned sb = lds0 + (t % ANS) * ASTAGE + wid * 1024;
#pragma unroll
    for (int i = 0; i < 3; ++i) glds16(dsrc[i] + t * AKB, sb + i * 8192);
  };
  issue(0); issue(1); issue(2);

  const int koff = r32 * 48 + hi * 16;
  const int k1off = r32 * 48 + 32;
  const int voff = AKB + (4 * hi + ((lane & 15) >> 2)) * 48 + ((lane >> 4) & 1) * 32 + (lane & 3) * 8;
  const int khoff = kh * (128 * 48);

  waitv<6>();
  barrier_raw();
  TS(1);
  bf16x8 qa0, qa1, qb0, qb1;
  {
    const LDS_AS char* qp = lds + ANS * ASTAGE + 64 + (qg * 64 + r32) * 48;
    qa0 = *(const LDS_AS bf16x8*)(qp + hi * 16);
    qa1 = *(const LDS_AS bf16x8*)(qp + 32);
    qb0 = *(const LDS_AS bf16x8*)(qp + 32 * 48 + hi * 16);
    qb1 = *(const LDS_AS bf16x8*)(qp + 32 * 48 + 32);
    if (hi) { qa1 = bf16x8{0, 0, 0, 0, 0, 0, 0, 0}; qb1 = qa1; }
  }
  {
    bf16x8 kf0 = *(const LDS_AS bf16x8*)(lds + koff), kf1 = *(const LDS_AS bf16x8*)(lds + k1off);
    f32x16 sa = __builtin_amdgcn_mfma_f32_32x32x16_bf16(kf0, qa0, f32x16{}, 0, 0, 0);
    f32x16 sb = __builtin_amdgcn_mfma_f32_32x32x16_bf16(kf0, qb0, f32x16{}, 0, 0, 0);
    sa = __builtin_amdgcn_mfma_f32_32x32x16_bf16(kf1, qa1, sa, 0, 0, 0);
    sb = __builtin_amdgcn_mfma_f32_32x32x16_bf16(kf1, qb1, sb, 0, 0, 0);
    float ma = sa[0], mb = sb[0];
#pragma unroll
    for (int r = 1; r < 16; ++r) { ma = fmaxf(ma, sa[r]); mb = fmaxf(mb, sb[r]); }
    ma = fmaxf(ma, __shfl_xor(ma, 32));
    mb = fmaxf(mb, __shfl_xor(mb, 32));
    const unsigned pk = pack2(-ma, -mb);
    u32x4 ta = __builtin_bit_cast(u32x4, qa1), tb = __builtin_bit_cast(u32x4, qb1);
    if (!hi) { ta[2] = pk & 0xffffu; tb[2] = pk >> 16; }
    qa1 = __builtin_bit_cast(bf16x8, ta);
    qb1 = __builtin_bit_cast(bf16x8, tb);
  }

  f32x16 oa = {}, obb = {};
  auto tbase = [&](int i) -> const LDS_AS char* { return lds + ((i >> 2) % ANS) * ASTAGE + khoff + (i & 3) * (32 * 48); };
  auto rdK = [&](int i, bf16x8& k0, bf16x8& k1) {
    const LDS_AS char* p = tbase(i);
    k0 = *(const LDS_AS bf16x8*)(p + koff);
    k1 = *(const LDS_AS bf16x8*)(p + k1off);
  };
  auto rdV = [&](int i, bf16x8& v0, bf16x8& v1) {
    const LDS_AS char* p = tbase(i) + voff;
    bf16x4 a0 = lds_tr(p), a1 = lds_tr(p + 384), a2 = lds_tr(p + 768), a3 = lds_tr(p + 1152);
    v0 = bf16x8{a0[0], a0[1], a0[2], a0[3], a1[0], a1[1], a1[2], a1[3]};
    v1 = bf16x8{a2[0], a2[1], a2[2], a2[3], a3[0], a3[1], a3[2], a3[3]};
  };
  auto expo = [&](const f32x16& s, int r0) -> bf16x8 {
    float p[8];
#pragma unroll
    for (int r = 0; r < 8; ++r) p[r] = __builtin_amdgcn_exp2f(s[r0 + r]);
    u32x4 pb = {pack2(p[0], p[1]), pack2(p[2], p[3]), pack2(p[4], p[5]), pack2(p[6], p[7])};
    return __builtin_bit_cast(bf16x8, pb);
  };
  bf16x8 kc0, kc1, kn0, kn1, vc0, vc1, vn0, vn1;
  f32x16 sca, scb, sna, snb;
  rdK(0, kc0, kc1);
  rdK(1, kn0, kn1);
  rdV(0, vc0, vc1);
  sca = __builtin_amdgcn_mfma_f32_32x32x16_bf16(kc0, qa0, f32x16{}, 0, 0, 0);
  scb = __builtin_amdgcn_mfma_f32_32x32x16_bf16(kc0, qb0, f32x16{}, 0, 0, 0);
  sca = __builtin_amdgcn_mfma_f32_32x32x16_bf16(kc1, qa1, sca, 0, 0, 0);
  scb = __builtin_amdgcn_mfma_f32_32x32x16_bf16(kc1, qb1, scb, 0, 0, 0);
  kc0 = kn0; kc1 = kn1;
  constexpr int NTILE = S / ATK;
#define SB0 __builtin_amdgcn_sched_barrier(0)
  for (int t = 0; t < NTILE; ++t) {
#pragma unroll
    for (int ii = 0; ii < 4; ++ii) {
      const int i = t * 4 + ii;
      if (ii == 2 && t + 1 < NTILE) {
        if (t + 2 < NTILE) waitv<3>(); else waitv<0>();
        barrier_raw();
        if (t + 3 < NTILE) issue(t + 3);
      }
      rdK(i + 2, kn0, kn1);
      rdV(i + 1, vn0, vn1);
      SB0;
      sna = __builtin_amdgcn_mfma_f32_32x32x16_bf16(kc0, qa0, f32x16{}, 0, 0, 0);
      sna = __builtin_amdgcn_mfma_f32_32x32x16_bf16(kc1, qa1, sna, 0, 0, 0);
      SB0;
      bf16x8 p = expo(sca, 0);
      SB0;
      oa = __builtin_amdgcn_mfma_f32_32x32x16_bf16(vc0, p, oa, 0, 0, 0);
      snb = __builtin_amdgcn_mfma_f32_32x32x16_bf16(kc0, qb0, f32x16{}, 0, 0, 0);
      SB0;
      p = expo(sca, 8);
      SB0;
      oa = __builtin_amdgcn_mfma_f32_32x32x16_bf16(vc1, p, oa, 0, 0, 0);
      snb = __builtin_amdgcn_mfma_f32_32x32x16_bf16(kc1, qb1, snb, 0, 0, 0);
      SB0;
      p = expo(scb, 0);
      SB0;
      obb = __builtin_amdgcn_mfma_f32_32x32x16_bf16(vc0, p, obb, 0, 0, 0);
      SB0;
      p = expo(scb, 8);
      SB0;
      obb = __builtin_amdgcn_mfma_f32_32x32x16_bf16(vc1, p, obb, 0, 0, 0);
      SB0;
      sca = sna; scb = snb; kc0 = kn0; kc1 = kn1; vc0 = vn0; vc1 = vn1;
    }
    if (t == 0) TS(2);
    if (t == 6) TS(3);
  }
  TS(4);
  barrier_raw();
  TS(5);
  {
    LDS_AS f32x4* ex = (LDS_AS f32x4*)(lds + qg * 8192);
    if (kh == 1) {
#pragma unroll
      for (int r = 0; r < 3; ++r) {
        ex[r * 64 + lane] = f32x4{oa[4 * r], oa[4 * r + 1], oa[4 * r + 2], oa[4 * r + 3]};
        ex[(3 + r) * 64 + lane] = f32x4{obb[4 * r], obb[4 * r + 1], obb[4 * r + 2], obb[4 * r + 3]};
      }
    }
    barrier_raw();
    if (kh == 1) { TSFLUSH(kid); return; }
#pragma unroll
    for (int r = 0; r < 3; ++r) {
      const f32x4 e = ex[r * 64 + lane], f = ex[(3 + r) * 64 + lane];
      oa[4 * r] += e[0]; oa[4 * r + 1] += e[1]; oa[4 * r + 2] += e[2]; oa[4 * r + 3] += e[3];
      obb[4 * r] += f[0]; obb[4 * r + 1] += f[1]; obb[4 * r + 2] += f[2]; obb[4 * r + 3] += f[3];
    }
  }
  const float la = __shfl(oa[8], r32 + 32), lb = __shfl(obb[8], r32 + 32);
  bool bad = !(la < 1.0e18f) || !(lb < 1.0e18f);
#pragma unroll
  for (int r = 0; r < 12; ++r) bad = bad || !(fabsf(oa[r]) < 3.0e38f) || !(fabsf(obb[r]) < 3.0e38f);
  if (__any(bad)) {
    const int q = q0 + 32 * hi;
    float qv[DH];
    {
      const u16* qp = Qh + (size_t)q * HP;
#pragma unroll
      for (int d = 0; d < DH; ++d) qv[d] = bf2f(qp[d]);
    }
    float m = -INFINITY, ls = 0.f, ov[DH];
#pragma unroll
    for (int d = 0; d < DH; ++d) ov[d] = 0.f;
    for (int key = 0; key < S; ++key) {
      const u16* kb = Kh + (size_t)key * HP;
      float sc = 0.f;
#pragma unroll
      for (int d = 0; d < DH; ++d) sc += qv[d] * bf2f(kb[d]);
      const float mn = fmaxf(m, sc);
      const float al = __builtin_amdgcn_exp2f(m - mn), pe = __builtin_amdgcn_exp2f(sc - mn);
      const u16* vb = Vh + (size_t)key * HP;
      ls = ls * al + pe;
#pragma unroll
      for (int d = 0; d < DH; ++d) ov[d] = ov[d] * al + pe * bf2f(vb[d]);
      m = mn;
    }
    const float rl = 1.f / ls;
    u16* op = O + (size_t)q * D + h * DH;
#pragma unroll
    for (int d = 0; d < DH; d += 4) {
      u32x2 w = {pack2(ov[d] * rl, ov[d + 1] * rl), pack2(ov[d + 2] * rl, ov[d + 3] * rl)};
      *(u32x2*)(op + d) = w;
    }
    return;
  }
  const float ra = 1.f / la, rbb = 1.f / lb;
  u16* op = O + (size_t)q0 * D + h * DH + 4 * hi;
#pragma unroll
  for (int gq = 0; gq < 3; ++gq) {
    if (gq < 2 || !hi) {
      u32x2 w = {pack2(oa[4 * gq] * ra, oa[4 * gq + 1] * ra), pack2(oa[4 * gq + 2] * ra, oa[4 * gq + 3] * ra)};
      *(u32x2*)(op + 8 * gq) = w;
      u32x2 w2 = {pack2(obb[4 * gq] * rbb, obb[4 * gq + 1] * rbb), pack2(obb[4 * gq + 2] * rbb, obb[4 * gq + 3] * rbb)};
      *(u32x2*)(op + 32 * D + 8 * gq) = w2;
    }
  }
  TS(6);
  TSFLUSH(kid);
}

#define QKV_BK 64
#define QKV_NS 4
#define GU_BK 64
#define GU_NS 3
#define RES_NS 8
extern "C" void kernel_launch(void* const* d_in, const int* in_sizes, int n_in, void* d_out, int out_size,
                              void* d_ws, size_t ws_size, hipStream_t stream) {
  const u16* x0 = (const u16*)d_in[0];
  const u16* wqkv = (const u16*)d_in[1];
  const u16* wo = (const u16*)d_in[2];
  const u16* wg = (const u16*)d_in[4];
  const u16* wu = (const u16*)d_in[5];
  const u16* wd = (const u16*)d_in[6];
  char* ws = (char*)d_ws;
  size_t off = 0;
  auto alloc = [&](size_t bytes) { char* p = ws + off; off += (bytes + 255) & ~(size_t)255; return p; };
  float2* stats = (float2*)alloc((size_t)S * NPART * 8);
  float* cs = (float*)alloc(5376 * 4);
  u16* QKVb = (u16*)alloc((size_t)3 * H * S * HP * 2 + 4096);
  u16* Ob = (u16*)alloc((size_t)S * D * 2);
  u16* xa = (u16*)alloc((size_t)S * D * 2);
  u16* xb = (u16*)alloc((size_t)S * D * 2);
  u16* Hb = (u16*)alloc((size_t)S * DF * 2);

  prep_kernel<<<256 + 84, 256, 0, stream>>>(x0, stats, wqkv, wg, wu, cs);

  const u16* xin = x0;
  for (int layer = 0; layer < 2; ++layer) {
    u16* x1 = xa;
    u16* x2 = layer == 0 ? xb : (u16*)d_out;
    {
      GemmArgs a{}; a.X = xin; a.W0 = wqkv; a.stats_in = stats; a.cs0 = cs; a.out0 = QKVb; a.kid = layer * 5 + 0;
      gemm_kernel<EPI_QKV, 128, 128, 120, 1, 640, QKV_BK, QKV_NS, 2, 1920, 16, 8><<<256, 512, 0, stream>>>(a);
    }
    attn_kernel<<<256, 512, 0, stream>>>(QKVb, Ob, layer * 5 + 1);
    {
      GemmArgs a{}; a.X = Ob; a.W0 = wo; a.res = xin; a.out0 = x1; a.stats_out = stats; a.kid = layer * 5 + 2;
      gemm_kernel<EPI_RES, 64, 80, 80, 1, 640, 64, RES_NS, 1, 640, 8, 4><<<256, 512, 0, stream>>>(a);
    }
    {
      GemmArgs a{}; a.X = x1; a.W0 = wg; a.W1 = wu; a.stats_in = stats; a.cs0 = cs + 1920; a.cs1 = cs + 1920 + DF;
      a.out0 = Hb; a.kid = layer * 5 + 3;
      gemm_kernel<EPI_GEGLU, 128, 112, 108, 2, 640, GU_BK, GU_NS, 2, DF, 16, 4><<<256, 512, 0, stream>>>(a);
    }
    {
      GemmArgs a{}; a.X = Hb; a.W0 = wd; a.res = x1; a.out0 = x2; a.stats_out = stats; a.kid = layer * 5 + 4;
      gemm_kernel<EPI_RES, 64, 80, 80, 1, DF, 64, RES_NS, 1, 640, 8, 4><<<256, 512, 0, stream>>>(a);
    }
    xin = x2;
  }
}
```

```cpp
#include <hip/hip_runtime.h>
#include <hip/hip_bf16.h>
#include <stdint.h>

typedef unsigned short u16;
using bf16x8 = __attribute__((ext_vector_type(8))) short;
using bf16x4 = __attribute__((ext_vector_type(4))) short;
using f32x4 = __attribute__((ext_vector_type(4))) float;
using f32x16 = __attribute__((ext_vector_type(16))) float;
using u32x2 = __attribute__((ext_vector_type(2))) unsigned;
using f32x2 = __attribute__((ext_vector_type(2))) float;
using u32x4 = __attribute__((ext_vector_type(4))) unsigned;
typedef short v4i16_t __attribute__((ext_vector_type(4)));
#define LDS_AS __attribute__((address_space(3)))

constexpr int S = 2048, D = 640, H = 32, DH = 20, DF = 1728;
constexpr int NPART = 8;
constexpr int PCOLS = D / NPART;
constexpr int HP = 24;

#define TSDECL
#define TS(i)
#define TSFLUSH(kid)

__device__ __forceinline__ float bf2f(u16 v) { return __uint_as_float(((unsigned)v) << 16); }
__device__ __forceinline__ unsigned pack2(float lo, float hi) {
  typedef __bf16 bf2_t __attribute__((ext_vector_type(2)));
  return __builtin_bit_cast(unsigned, __builtin_convertvector(f32x2{lo, hi}, bf2_t));
}
__device__ __forceinline__ float lo2f(unsigned u) { return __uint_as_float(u << 16); }
__device__ __forceinline__ float hi2f(unsigned u) { return __uint_as_float(u & 0xffff0000u); }

__device__ __forceinline__ void glds16(const void* g, unsigned lb) {
  unsigned sv;
  asm volatile("s_mov_b32 %0, m0\n\ts_mov_b32 m0, %2\n\ts_nop 0\n\tglobal_load_lds_dwordx4 %1, off\n\ts_mov_b32 m0, %0"
               : "=&s"(sv) : "v"(g), "s"(lb) : "memory");
}
__device__ __forceinline__ bf16x4 lds_tr(const LDS_AS char* p) {
  return __builtin_amdgcn_ds_read_tr16_b64_v4i16((LDS_AS v4i16_t*)p);
}
template <int N> __device__ __forceinline__ void waitv() {
  static_assert(N >= 0 && N < 64, "");
  asm volatile("s_waitcnt vmcnt(%0)" :: "n"(N) : "memory");
}
__device__ __forceinline__ void barrier_raw() {
  asm volatile("" ::: "memory");
  __builtin_amdgcn_s_waitcnt(0xC07F);
  __builtin_amdgcn_s_barrier();
  asm volatile("" ::: "memory");
}

struct PrepArgs {
  const u16* x; float2* stats;
  const u16* W[5]; u16* P[5]; float* csp[3];
};
constexpr int NKC = 10;
template <int BNS, int NFT, int LD>
__device__ __forceinline__ void repack_tile(const u16* __restrict__ W, u16* __restrict__ P, float* __restrict__ csp,
                                            int cb, int kc, int nkc, int ncb, u16 (*t)[132]) {
  const int tid = threadIdx.x;
  constexpr int Q4 = BNS / 4, BNP = NFT * 16;
  for (int i = tid; i < 64 * (BNP / 4); i += 256) {
    const int r = i / (BNP / 4), c4 = i - r * (BNP / 4);
    u32x2 v = {0u, 0u};
    if (c4 < Q4) v = *(const u32x2*)(W + (size_t)(kc * 64 + r) * LD + cb * BNS + c4 * 4);
    *(u32x2*)&t[r][c4 * 4] = v;
  }
  __syncthreads();
  u16* dst = P + ((size_t)cb * (nkc * 2) + kc * 2) * (NFT * 512);
  for (int i = tid; i < 2 * NFT * 64; i += 256) {
    const int lane = i & 63, f = i >> 6, kk = f / NFT, nf = f - kk * NFT;
    const int r0 = kk * 32 + 8 * (lane >> 4), c = nf * 16 + (lane & 15);
    u32x4 o;
#pragma unroll
    for (int j = 0; j < 4; ++j) o[j] = (unsigned)t[r0 + 2 * j][c] | ((unsigned)t[r0 + 2 * j + 1][c] << 16);
    *(u32x4*)(dst + (size_t)i * 8) = o;
  }
  if (csp != nullptr && tid < BNP) {
    float sum = 0.f;
#pragma unroll 8
    for (int r = 0; r < 64; ++r) sum += bf2f(t[r][tid]);
    csp[(size_t)kc * (ncb * BNP) + cb * BNP + tid] = sum;
  }
}
__global__ __launch_bounds__(256) void prep_kernel(PrepArgs a) {
  __shared__ __attribute__((aligned(16))) u16 t[64][132];
  const int tid = threadIdx.x, lane = tid & 63, w = tid >> 6;
  int b = blockIdx.x;
  if (b < 160) { repack_tile<120, 8, 1920>(a.W[0], a.P[0], a.csp[0], b & 15, b >> 4, NKC, 16, t); return; }
  b -= 160;
  if (b < 160) { repack_tile<108, 7, DF>(a.W[1], a.P[1], a.csp[1], b & 15, b >> 4, NKC, 16, t); return; }
  b -= 160;
  if (b < 160) { repack_tile<108, 7, DF>(a.W[2], a.P[2], a.csp[2], b & 15, b >> 4, NKC, 16, t); return; }
  b -= 160;
  if (b < 80) { repack_tile<80, 5, D>(a.W[3], a.P[3], nullptr, b & 7, b >> 3, NKC, 8, t); return; }
  b -= 80;
  if (b < 216) { repack_tile<80, 5, D>(a.W[4], a.P[4], nullptr, b & 7, b >> 3, 27, 8, t); return; }
  b -= 216;
  for (int rr = 0; rr < 2; ++rr) {
    const int row = b * 8 + w * 2 + rr;
    const unsigned* p = (const unsigned*)(a.x + (size_t)row * D + lane * 10);
    float v[10];
#pragma unroll
    for (int i = 0; i < 5; ++i) { unsigned u = p[i]; v[2 * i] = lo2f(u); v[2 * i + 1] = hi2f(u); }
    float s = 0.f;
#pragma unroll
    for (int i = 0; i < 10; ++i) s += v[i];
    s += __shfl_xor(s, 1); s += __shfl_xor(s, 2); s += __shfl_xor(s, 4);
    const float mu = s * (1.f / PCOLS);
    float q = 0.f;
#pragma unroll
    for (int i = 0; i < 10; ++i) { float d = v[i] - mu; q += d * d; }
    q += __shfl_xor(q, 1); q += __shfl_xor(q, 2); q += __shfl_xor(q, 4);
    if ((lane & 7) == 0) a.stats[row * NPART + (lane >> 3)] = make_float2(s, q);
  }
}

enum { EPI_QKV = 0, EPI_RES = 1, EPI_GEGLU = 2 };

struct GemmArgs {
  const u16* X;
  const u16* W0;
  const u16* W1;
  const float2* stats_in;
  const float* cs0;
  const float* cs1;
  const u16* res;
  u16* out0;
  float2* stats_out;
  int kid;
  int pad_;
};

constexpr int NLW = 4, NCW = 4;
template <int EPI, int BM, int NFT, int BNS, int NW, int K, int NS, int WN, int NCOLS, int NCB, int CPX>
__global__ __launch_bounds__(512) void gemm_kernel(GemmArgs a) {
  TSDECL; TS(0);
  constexpr int BK = 64, NK = K / BK, NU = K / 32, BNP = NFT * 16;
  constexpr int XB = BM * 128;
  constexpr int WB = 2 * NFT * 1024;
  constexpr int STAGE = XB + NW * WB;
  constexpr int XI = XB / 1024, WI = WB / 1024, T = XI + NW * WI;
  constexpr int LPA = (T + NLW - 1) / NLW, NBIG = T - (LPA - 1) * NLW;
  constexpr int WM = NCW / WN;
  constexpr int MF = BM / WM / 16;
  constexpr int NF = (NW == 2) ? NFT : NFT / WN;
  constexpr int RPX = 32 / CPX, NCG = NCB / CPX;
  constexpr int TABB = BM * 8 + NW * BNP * 4;
  static_assert(NS * STAGE + TABB <= 163840, "LDS");
  __shared__ __attribute__((aligned(16))) char smem[NS * STAGE + TABB];

  const int tid = threadIdx.x, lane = tid & 63;
  const int wid = __builtin_amdgcn_readfirstlane(tid >> 6);
  const int xcd = blockIdx.x & 7, ob = blockIdx.x >> 3;
  const int rb = (xcd / NCG) * RPX + ob / CPX, cb = (xcd % NCG) * CPX + ob % CPX;
  const int m0 = rb * BM, n0 = cb * BNS;
  LDS_AS char* lds = (LDS_AS char*)smem;
  const unsigned lds0 = (unsigned)(uintptr_t)lds;
  LDS_AS f32x2* sst = (LDS_AS f32x2*)(lds + NS * STAGE);
  LDS_AS float* scs = (LDS_AS float*)(lds + NS * STAGE + BM * 8);

  if (wid >= NCW) {
    const int lw = wid - NCW;
    const char* src[LPA];
    unsigned strd[LPA];
#pragma unroll
    for (int it = 0; it < LPA; ++it) {
      const int t = it * NLW + lw;
      if (t < XI) {
        const int c = t * 64 + lane, row = c >> 3, slot = c & 7;
        src[it] = (const char*)(a.X + (size_t)(m0 + row) * K + (slot ^ (row & 7)) * 8);
        strd[it] = BK * 2;
      } else {
        const int tw = t - XI, w = (NW == 2 && tw >= WI) ? 1 : 0;
        src[it] = (const char*)(w ? a.W1 : a.W0) + (size_t)cb * (NK * WB) + (tw - w * WI) * 1024 + lane * 16;
        strd[it] = WB;
      }
    }
    auto issue = [&](int kt) {
      const unsigned sb = lds0 + (kt % NS) * STAGE + lw * 1024;
#pragma unroll
      for (int it = 0; it < LPA; ++it) {
        if (it < LPA - 1 || lw < NBIG) {
          glds16(src[it], sb + it * (NLW * 1024));
          src[it] += strd[it];
        }
      }
    };
#pragma unroll
    for (int s = 0; s < NS; ++s) if (s < NK) issue(s);
    if (lw < NBIG) waitv<(NS - 1) * LPA>(); else waitv<(NS - 1) * (LPA - 1)>();
    barrier_raw();
    TS(1);
    for (int j = 0; j < NK - 1; ++j) {
      if (j + NS <= NK) { if (lw < NBIG) waitv<(NS - 2) * LPA>(); else waitv<(NS - 2) * (LPA - 1)>(); }
      else {
        const int rem = NK - 2 - j;
        if (rem >= 5) { if (lw < NBIG) waitv<5 * LPA>(); else waitv<5 * (LPA - 1)>(); }
        else if (rem == 4) { if (lw < NBIG) waitv<4 * LPA>(); else waitv<4 * (LPA - 1)>(); }
        else if (rem == 3) { if (lw < NBIG) waitv<3 * LPA>(); else waitv<3 * (LPA - 1)>(); }
        else if (rem == 2) { if (lw < NBIG) waitv<2 * LPA>(); else waitv<2 * (LPA - 1)>(); }
        else if (rem == 1) { if (lw < NBIG) waitv<LPA>(); else waitv<LPA - 1>(); }
        else waitv<0>();
      }
      barrier_raw();
      if (j + NS < NK) issue(j + NS);
    }
    TS(2);
    if constexpr (EPI != EPI_RES) {
      barrier_raw();
      barrier_raw();
    }
  } else {
    const int wm = wid / WN, wn = wid % WN;
    const int wsel = (NW == 2) ? wn : 0;
    const int nfb = (NW == 2) ? 0 : wn * NF;
    const int g = lane >> 4;
    u32x2 resr[NF][MF];
    if constexpr (EPI != EPI_RES) {
      if (tid < BM) {
        float4 st[NPART / 2];
        const float4* sp = (const float4*)(a.stats_in + (size_t)(m0 + tid) * NPART);
#pragma unroll
        for (int i = 0; i < NPART / 2; ++i) st[i] = sp[i];
        float s = 0.f;
#pragma unroll
        for (int i = 0; i < NPART / 2; ++i) s += st[i].x + st[i].z;
        const float mean = s * (1.f / D);
        float m2 = 0.f;
#pragma unroll
        for (int i = 0; i < NPART / 2; ++i) {
          const float d0 = st[i].x * (1.f / PCOLS) - mean, d1 = st[i].z * (1.f / PCOLS) - mean;
          m2 += st[i].y + st[i].w + PCOLS * (d0 * d0 + d1 * d1);
        }
        const float rstd = 1.f / sqrtf(m2 * (1.f / D) + 1e-5f);
        sst[tid] = f32x2{mean, rstd};
      }
      if (tid < NW * BNP) {
        const int w = tid >= BNP ? 1 : 0, c = tid - w * BNP;
        const float* cp = (w ? a.cs1 : a.cs0) + cb * BNP + c;
        float v[NKC];
#pragma unroll
        for (int i = 0; i < NKC; ++i) v[i] = cp[i * (NCB * BNP)];
        float s = 0.f;
#pragma unroll
        for (int i = 0; i < NKC; ++i) s += v[i];
        scs[tid] = s;
      }
    } else {
#pragma unroll
      for (int j = 0; j < MF; ++j)
#pragma unroll
        for (int i = 0; i < NF; ++i) {
          const int m = m0 + wm * (MF * 16) + j * 16 + (lane & 15);
          const int n = n0 + (nfb + i) * 16 + 4 * g;
          resr[i][j] = *(const u32x2*)(a.res + (size_t)m * D + n);
        }
    }
    f32x4 acc[NF][MF];
#pragma unroll
    for (int i = 0; i < NF; ++i)
#pragma unroll
      for (int j = 0; j < MF; ++j) acc[i][j] = f32x4{0.f, 0.f, 0.f, 0.f};

    const int xrow = wm * (MF * 16) + (lane & 15);
    int xo[2];
#pragma unroll
    for (int kk = 0; kk < 2; ++kk) xo[kk] = xrow * 128 + (((kk * 4 + g) ^ (lane & 7)) * 16);
    const int wo = XB + wsel * WB + nfb * 1024 + lane * 16;
    bf16x8 xf[2][MF], wf[2][NF];
    auto rdfrags = [&](int u, int slot) {
      const LDS_AS char* sb = lds + ((u >> 1) % NS) * STAGE;
      const int kk = u & 1;
#pragma unroll
      for (int j = 0; j < MF; ++j) xf[slot][j] = *(const LDS_AS bf16x8*)(sb + xo[kk] + j * 2048);
#pragma unroll
      for (int i = 0; i < NF; ++i) wf[slot][i] = *(const LDS_AS bf16x8*)(sb + wo + (kk * NFT + i) * 1024);
    };
    auto mfmas = [&](int slot) {
#pragma unroll
      for (int i = 0; i < NF; ++i)
#pragma unroll
        for (int j = 0; j < MF; ++j)
          acc[i][j] = __builtin_amdgcn_mfma_f32_16x16x32_bf16(wf[slot][i], xf[slot][j], acc[i][j], 0, 0, 0);
    };
    auto interleave = [&]() {
      constexpr int NR = MF + NF, NM = MF * NF, NP = NR < NM ? NR : NM;
#pragma unroll
      for (int r = 0; r < NP; ++r) {
        __builtin_amdgcn_sched_group_barrier(0x008, 1, 0);
        __builtin_amdgcn_sched_group_barrier(0x100, 1, 0);
      }
      if constexpr (NM > NP) __builtin_amdgcn_sched_group_barrier(0x008, NM - NP, 0);
      if constexpr (NR > NP) __builtin_amdgcn_sched_group_barrier(0x100, NR - NP, 0);
    };
    barrier_raw();
    TS(1);
    rdfrags(0, 0);
    for (int u = 0; u < NU - 2; u += 2) {
      __builtin_amdgcn_s_waitcnt(0xC07F);
      __builtin_amdgcn_sched_barrier(0);
      rdfrags(u + 1, 1);
      mfmas(0);
      interleave();
      __builtin_amdgcn_sched_barrier(0);
      barrier_raw();
      __builtin_amdgcn_sched_barrier(0);
      rdfrags(u + 2, 0);
      mfmas(1);
      interleave();
      __builtin_amdgcn_sched_barrier(0);
    }
    __builtin_amdgcn_s_waitcnt(0xC07F);
    __builtin_amdgcn_sched_barrier(0);
    rdfrags(NU - 1, 1);
    mfmas(0);
    interleave();
    __builtin_amdgcn_sched_barrier(0);
    mfmas(1);
    TS(2);

    if constexpr (EPI == EPI_QKV) {
      barrier_raw();
      TS(3);
#pragma unroll
      for (int j = 0; j < MF; ++j) {
        const int ml = wm * (MF * 16) + j * 16 + (lane & 15);
        const f32x2 st = sst[ml];
#pragma unroll
        for (int i = 0; i < NF; ++i) {
          const int nl = (nfb + i) * 16 + 4 * g;
          const float sc = (n0 + nl < D) ? st.y * (0.22360679774997896f * 1.4426950408889634f) : st.y;
          const f32x4 c = *(const LDS_AS f32x4*)(scs + nl);
          u32x2 o = {pack2(sc * (acc[i][j][0] - st.x * c[0]), sc * (acc[i][j][1] - st.x * c[1])),
                     pack2(sc * (acc[i][j][2] - st.x * c[2]), sc * (acc[i][j][3] - st.x * c[3]))};
          *(LDS_AS u32x2*)(lds + ml * 264 + nl * 2) = o;
        }
      }
      barrier_raw();
    } else if constexpr (EPI == EPI_GEGLU) {
      barrier_raw();
      TS(3);
#pragma unroll
      for (int j = 0; j < MF; ++j) {
        const int ml = wm * (MF * 16) + j * 16 + (lane & 15);
        const f32x2 st = sst[ml];
#pragma unroll
        for (int i = 0; i < NF; ++i) {
          const int nl = i * 16 + 4 * g;
          const f32x4 c = *(const LDS_AS f32x4*)(scs + wsel * BNP + nl);
          f32x4 o = {st.y * (acc[i][j][0] - st.x * c[0]), st.y * (acc[i][j][1] - st.x * c[1]),
                     st.y * (acc[i][j][2] - st.x * c[2]), st.y * (acc[i][j][3] - st.x * c[3])};
          *(LDS_AS f32x4*)(lds + wsel * (BM * 464) + ml * 464 + nl * 4) = o;
        }
      }
      barrier_raw();
    } else {
      TS(3);
#pragma unroll
      for (int j = 0; j < MF; ++j) {
        const int m = m0 + wm * (MF * 16) + j * 16 + (lane & 15);
        float xv[NF][4];
        float s = 0.f;
#pragma unroll
        for (int i = 0; i < NF; ++i) {
          const int n = n0 + (nfb + i) * 16 + 4 * g;
          const u32x2 r = resr[i][j];
          u32x2 o = {pack2(lo2f(r[0]) + acc[i][j][0], hi2f(r[0]) + acc[i][j][1]),
                     pack2(lo2f(r[1]) + acc[i][j][2], hi2f(r[1]) + acc[i][j][3])};
          *(u32x2*)(a.out0 + (size_t)m * D + n) = o;
          xv[i][0] = lo2f(o[0]); xv[i][1] = hi2f(o[0]); xv[i][2] = lo2f(o[1]); xv[i][3] = hi2f(o[1]);
          s += (xv[i][0] + xv[i][1]) + (xv[i][2] + xv[i][3]);
        }
        s += __shfl_xor(s, 16); s += __shfl_xor(s, 32);
        const float mu = s * (1.f / PCOLS);
        float q = 0.f;
#pragma unroll
        for (int i = 0; i < NF; ++i)
#pragma unroll
          for (int r = 0; r < 4; ++r) { const float d = xv[i][r] - mu; q += d * d; }
        q += __shfl_xor(q, 16); q += __shfl_xor(q, 32);
        if (lane < 16) a.stats_out[(size_t)m * NPART + cb] = make_float2(s, q);
      }
    }
  }
  TS(4);
  if constexpr (EPI == EPI_QKV) {
#pragma unroll
    for (int p = 0; p < 5; ++p) {
      const int slot = p * 512 + tid;
      if (slot < 6 * 384) {
        const int hh = slot / 384, r = slot - hh * 384, m = r / 3, j = r - 3 * m;
        const int gh = cb * 6 + hh;
        const LDS_AS char* sp = lds + m * 264 + hh * 40 + j * 16;
        const u32x2 v0 = *(const LDS_AS u32x2*)sp;
        u32x2 v1 = *(const LDS_AS u32x2*)(sp + 8);
        if (j == 2) v1 = u32x2{gh < H ? 0u : 0x00003f80u, 0u};
        *(u32x4*)((char*)a.out0 + ((size_t)gh * S + m0) * (HP * 2) + r * 16) = u32x4{v0[0], v0[1], v1[0], v1[1]};
      }
    }
  } else if constexpr (EPI == EPI_GEGLU) {
#pragma unroll
    for (int p = 0; p < 4; ++p) {
      const int slot = p * 512 + tid;
      if (slot < BM * 14) {
        const int m = slot / 14, s8 = slot - m * 14;
        const LDS_AS char* gp = lds + m * 464 + s8 * 32;
        const f32x4 g0 = *(const LDS_AS f32x4*)gp, g1 = *(const LDS_AS f32x4*)(gp + 16);
        const f32x4 u0 = *(const LDS_AS f32x4*)(gp + BM * 464), u1 = *(const LDS_AS f32x4*)(gp + BM * 464 + 16);
        float hv[8];
#pragma unroll
        for (int r = 0; r < 8; ++r) {
          const float gv = r < 4 ? g0[r & 3] : g1[r & 3], uv = r < 4 ? u0[r & 3] : u1[r & 3];
          const float z = gv * (1.f + 0.044715f * gv * gv) * (2.f * 0.7978845608028654f * 1.4426950408889634f);
          const float e = __builtin_amdgcn_exp2f(-z);
          hv[r] = gv * __builtin_amdgcn_rcpf(1.f + e) * uv;
        }
        u16* op = a.out0 + (size_t)(m0 + m) * NCOLS + n0 + s8 * 8;
        const u32x2 o0 = {pack2(hv[0], hv[1]), pack2(hv[2], hv[3])};
        const u32x2 o1 = {pack2(hv[4], hv[5]), pack2(hv[6], hv[7])};
        if (s8 < 13) {
          *(u32x2*)op = o0; *(u32x2*)(op + 4) = o1;
        } else {
          *(u32x2*)op = o0;
        }
      }
    }
  }
  TS(5);
  TSFLUSH(a.kid);
}

constexpr int ATK = 256, ANS = 4, AKB = ATK * HP * 2, ASTAGE = 2 * AKB;
struct AFr { bf16x8 k0, k1; };
__global__ __launch_bounds__(512) void attn_kernel(const u16* __restrict__ QKV, u16* __restrict__ O, int kid) {
  TSDECL; TS(0);
  __shared__ __attribute__((aligned(16))) char smem[ANS * ASTAGE + 64 + 12288];
  LDS_AS char* lds = (LDS_AS char*)smem;
  const unsigned lds0 = (unsigned)(uintptr_t)lds;
  const int tid = threadIdx.x, lane = tid & 63;
  const int wid = __builtin_amdgcn_readfirstlane(tid >> 6);
  const int qg = wid & 3, kh = wid >> 2;
  const int b = blockIdx.x;
  const int h = (b & 7) + 8 * (b >> 6), qb = (b >> 3) & 7;
  const int r32 = lane & 31, hi = lane >> 5;
  const int q0 = qb * 256 + qg * 64 + r32;
  const u16* Qh = QKV + (size_t)h * S * HP;
  const u16* Kh = QKV + (size_t)(H + h) * S * HP;
  const u16* Vh = QKV + (size_t)(2 * H + h) * S * HP;

  {
    const char* qs = (const char*)(Qh + (size_t)qb * 256 * HP) + lane * 16;
    glds16(qs + wid * 1024, lds0 + ANS * ASTAGE + 64 + wid * 1024);
    if (wid < 4) glds16(qs + (8 + wid) * 1024, lds0 + ANS * ASTAGE + 64 + (8 + wid) * 1024);
  }
  const char* dsrc[3];
#pragma unroll
  for (int i = 0; i < 3; ++i) {
    const int p = wid + 8 * i;
    dsrc[i] = (p < 12 ? (const char*)Kh + p * 1024 : (const char*)Vh + (p - 12) * 1024) + lane * 16;
  }
  auto issue = [&](int t) {
    const unsigned sb = lds0 + (t % ANS) * ASTAGE + wid * 1024;
#pragma unroll
    for (int i = 0; i < 3; ++i) glds16(dsrc[i] + t * AKB, sb + i * 8192);
  };
  issue(0); issue(1); issue(2);

  const int koff = r32 * 48 + hi * 16;
  const int k1off = r32 * 48 + 32;
  const int voff = AKB + (4 * hi + ((lane & 15) >> 2)) * 48 + ((lane >> 4) & 1) * 32 + (lane & 3) * 8;
  const int khoff = kh * (128 * 48);

  waitv<6>();
  barrier_raw();
  TS(1);
  bf16x8 qa0, qa1, qb0, qb1;
  {
    const LDS_AS char* qp = lds + ANS * ASTAGE + 64 + (qg * 64 + r32) * 48;
    qa0 = *(const LDS_AS bf16x8*)(qp + hi * 16);
    qa1 = *(const LDS_AS bf16x8*)(qp + 32);
    qb0 = *(const LDS_AS bf16x8*)(qp + 32 * 48 + hi * 16);
    qb1 = *(const LDS_AS bf16x8*)(qp + 32 * 48 + 32);
    if (hi) { qa1 = bf16x8{0, 0, 0, 0, 0, 0, 0, 0}; qb1 = qa1; }
  }
  {
    bf16x8 kf0 = *(const LDS_AS bf16x8*)(lds + koff), kf1 = *(const LDS_AS bf16x8*)(lds + k1off);
    f32x16 sa = __builtin_amdgcn_mfma_f32_32x32x16_bf16(kf0, qa0, f32x16{}, 0, 0, 0);
    f32x16 sb = __builtin_amdgcn_mfma_f32_32x32x16_bf16(kf0, qb0, f32x16{}, 0, 0, 0);
    sa = __builtin_amdgcn_mfma_f32_32x32x16_bf16(kf1, qa1, sa, 0, 0, 0);
    sb = __builtin_amdgcn_mfma_f32_32x32x16_bf16(kf1, qb1, sb, 0, 0, 0);
    float ma = sa[0], mb = sb[0];
#pragma unroll
    for (int r = 1; r < 16; ++r) { ma = fmaxf(ma, sa[r]); mb = fmaxf(mb, sb[r]); }
    ma = fmaxf(ma, __shfl_xor(ma, 32));
    mb = fmaxf(mb, __shfl_xor(mb, 32));
    const unsigned pk = pack2(-ma, -mb);
    u32x4 ta = __builtin_bit_cast(u32x4, qa1), tb = __builtin_bit_cast(u32x4, qb1);
    if (!hi) { ta[2] = pk & 0xffffu; tb[2] = pk >> 16; }
    qa1 = __builtin_bit_cast(bf16x8, ta);
    qb1 = __builtin_bit_cast(bf16x8, tb);
  }

  f32x16 oa = {}, obb = {};
  auto tbase = [&](int i) -> const LDS_AS char* { return lds + ((i >> 2) % ANS) * ASTAGE + khoff + (i & 3) * (32 * 48); };
  auto rdK = [&](int i, bf16x8& k0, bf16x8& k1) {
    const LDS_AS char* p = tbase(i);
    k0 = *(const LDS_AS bf16x8*)(p + koff);
    k1 = *(const LDS_AS bf16x8*)(p + k1off);
  };
  auto rdV = [&](int i, bf16x8& v0, bf16x8& v1) {
    const LDS_AS char* p = tbase(i) + voff;
    bf16x4 a0 = lds_tr(p), a1 = lds_tr(p + 384), a2 = lds_tr(p + 768), a3 = lds_tr(p + 1152);
    v0 = bf16x8{a0[0], a0[1], a0[2], a0[3], a1[0], a1[1], a1[2], a1[3]};
    v1 = bf16x8{a2[0], a2[1], a2[2], a2[3], a3[0], a3[1], a3[2], a3[3]};
  };
  auto expo = [&](const f32x16& s, int r0) -> bf16x8 {
    float p[8];
#pragma unroll
    for (int r = 0; r < 8; ++r) p[r] = __builtin_amdgcn_exp2f(s[r0 + r]);
    u32x4 pb = {pack2(p[0], p[1]), pack2(p[2], p[3]), pack2(p[4], p[5]), pack2(p[6], p[7])};
    return __builtin_bit_cast(bf16x8, pb);
  };
  auto expq = [&](const f32x16& s, int r0, unsigned& w0, unsigned& w1) {
    const float p0 = __builtin_amdgcn_exp2f(s[r0]), p1 = __builtin_amdgcn_exp2f(s[r0 + 1]);
    const float p2 = __builtin_amdgcn_exp2f(s[r0 + 2]), p3 = __builtin_amdgcn_exp2f(s[r0 + 3]);
    w0 = pack2(p0, p1); w1 = pack2(p2, p3);
  };
  auto mk = [&](const unsigned* w) -> bf16x8 { return __builtin_bit_cast(bf16x8, u32x4{w[0], w[1], w[2], w[3]}); };
  bf16x8 kc0, kc1, kn0, kn1, vc0, vc1, vn0, vn1;
  bf16x8 pb2 = {0, 0, 0, 0, 0, 0, 0, 0}, vp1 = {0, 0, 0, 0, 0, 0, 0, 0};
  f32x16 sca, scb, sna, snb;
  rdK(0, kc0, kc1);
  rdK(1, kn0, kn1);
  rdV(0, vc0, vc1);
  sca = __builtin_amdgcn_mfma_f32_32x32x16_bf16(kc0, qa0, f32x16{}, 0, 0, 0);
  scb = __builtin_amdgcn_mfma_f32_32x32x16_bf16(kc0, qb0, f32x16{}, 0, 0, 0);
  sca = __builtin_amdgcn_mfma_f32_32x32x16_bf16(kc1, qa1, sca, 0, 0, 0);
  scb = __builtin_amdgcn_mfma_f32_32x32x16_bf16(kc1, qb1, scb, 0, 0, 0);
  kc0 = kn0; kc1 = kn1;
  constexpr int NTILE = S / ATK;
#define SB0 __builtin_amdgcn_sched_barrier(0)
  for (int t = 0; t < NTILE; ++t) {
#pragma unroll
    for (int ii = 0; ii < 4; ++ii) {
      const int i = t * 4 + ii;
      if (ii == 2 && t + 1 < NTILE) {
        if (t + 2 < NTILE) waitv<3>(); else waitv<0>();
        barrier_raw();
        if (t + 3 < NTILE) issue(t + 3);
      }
      unsigned pw[4];
      SB0;
      obb = __builtin_amdgcn_mfma_f32_32x32x16_bf16(vp1, pb2, obb, 0, 0, 0);
      rdK(i + 2, kn0, kn1);
      expq(sca, 0, pw[0], pw[1]);
      SB0;
      sna = __builtin_amdgcn_mfma_f32_32x32x16_bf16(kc0, qa0, f32x16{}, 0, 0, 0);
      rdV(i + 1, vn0, vn1);
      expq(sca, 4, pw[2], pw[3]);
      SB0;
      oa = __builtin_amdgcn_mfma_f32_32x32x16_bf16(vc0, mk(pw), oa, 0, 0, 0);
      expq(sca, 8, pw[0], pw[1]);
      SB0;
      sna = __builtin_amdgcn_mfma_f32_32x32x16_bf16(kc1, qa1, sna, 0, 0, 0);
      expq(sca, 12, pw[2], pw[3]);
      SB0;
      oa = __builtin_amdgcn_mfma_f32_32x32x16_bf16(vc1, mk(pw), oa, 0, 0, 0);
      expq(scb, 0, pw[0], pw[1]);
      SB0;
      snb = __builtin_amdgcn_mfma_f32_32x32x16_bf16(kc0, qb0, f32x16{}, 0, 0, 0);
      expq(scb, 4, pw[2], pw[3]);
      SB0;
      obb = __builtin_amdgcn_mfma_f32_32x32x16_bf16(vc0, mk(pw), obb, 0, 0, 0);
      expq(scb, 8, pw[0], pw[1]);
      SB0;
      snb = __builtin_amdgcn_mfma_f32_32x32x16_bf16(kc1, qb1, snb, 0, 0, 0);
      expq(scb, 12, pw[2], pw[3]);
      SB0;
      pb2 = mk(pw); vp1 = vc1;
      sca = sna; scb = snb; kc0 = kn0; kc1 = kn1; vc0 = vn0; vc1 = vn1;
    }
    if (t == 0) TS(2);
    if (t == 6) TS(3);
  }
  obb = __builtin_amdgcn_mfma_f32_32x32x16_bf16(vp1, pb2, obb, 0, 0, 0);
  TS(4);
  barrier_raw();
  TS(5);
  {
    LDS_AS f32x4* ex = (LDS_AS f32x4*)(lds + qg * 8192);
    if (kh == 1) {
#pragma unroll
      for (int r = 0; r < 3; ++r) {
        ex[r * 64 + lane] = f32x4{oa[4 * r], oa[4 * r + 1], oa[4 * r + 2], oa[4 * r + 3]};
        ex[(3 + r) * 64 + lane] = f32x4{obb[4 * r], obb[4 * r + 1], obb[4 * r + 2], obb[4 * r + 3]};
      }
    }
    barrier_raw();
    if (kh == 1) { TSFLUSH(kid); return; }
#pragma unroll
    for (int r = 0; r < 3; ++r) {
      const f32x4 e = ex[r * 64 + lane], f = ex[(3 + r) * 64 + lane];
      oa[4 * r] += e[0]; oa[4 * r + 1] += e[1]; oa[4 * r + 2] += e[2]; oa[4 * r + 3] += e[3];
      obb[4 * r] += f[0]; obb[4 * r + 1] += f[1]; obb[4 * r + 2] += f[2]; obb[4 * r + 3] += f[3];
    }
  }
  const float la = __shfl(oa[8], r32 + 32), lb = __shfl(obb[8], r32 + 32);
  bool bad = !(la < 1.0e18f) || !(lb < 1.0e18f);
#pragma unroll
  for (int r = 0; r < 12; ++r) bad = bad || !(fabsf(oa[r]) < 3.0e38f) || !(fabsf(obb[r]) < 3.0e38f);
  if (__any(bad)) {
    const int q = q0 + 32 * hi;
    float qv[DH];
    {
      const u16* qp = Qh + (size_t)q * HP;
#pragma unroll
      for (int d = 0; d < DH; ++d) qv[d] = bf2f(qp[d]);
    }
    float m = -INFINITY, ls = 0.f, ov[DH];
#pragma unroll
    for (int d = 0; d < DH; ++d) ov[d] = 0.f;
    for (int key = 0; key < S; ++key) {
      const u16* kb = Kh + (size_t)key * HP;
      float sc = 0.f;
#pragma unroll
      for (int d = 0; d < DH; ++d) sc += qv[d] * bf2f(kb[d]);
      const float mn = fmaxf(m, sc);
      const float al = __builtin_amdgcn_exp2f(m - mn), pe = __builtin_amdgcn_exp2f(sc - mn);
      const u16* vb = Vh + (size_t)key * HP;
      ls = ls * al + pe;
#pragma unroll
      for (int d = 0; d < DH; ++d) ov[d] = ov[d] * al + pe * bf2f(vb[d]);
      m = mn;
    }
    const float rl = 1.f / ls;
    u16* op = O + (size_t)q * D + h * DH;
#pragma unroll
    for (int d = 0; d < DH; d += 4) {
      u32x2 w = {pack2(ov[d] * rl, ov[d + 1] * rl), pack2(ov[d + 2] * rl, ov[d + 3] * rl)};
      *(u32x2*)(op + d) = w;
    }
    return;
  }
  const float ra = 1.f / la, rbb = 1.f / lb;
  u16* op = O + (size_t)q0 * D + h * DH + 4 * hi;
#pragma unroll
  for (int gq = 0; gq < 3; ++gq) {
    if (gq < 2 || !hi) {
      u32x2 w = {pack2(oa[4 * gq] * ra, oa[4 * gq + 1] * ra), pack2(oa[4 * gq + 2] * ra, oa[4 * gq + 3] * ra)};
      *(u32x2*)(op + 8 * gq) = w;
      u32x2 w2 = {pack2(obb[4 * gq] * rbb, obb[4 * gq + 1] * rbb), pack2(obb[4 * gq + 2] * rbb, obb[4 * gq + 3] * rbb)};
      *(u32x2*)(op + 32 * D + 8 * gq) = w2;
    }
  }
  TS(6);
  TSFLUSH(kid);
}

#define QKV_NS 4
#define GU_NS 3
#define RES_NS 6
extern "C" void kernel_launch(void* const* d_in, const int* in_sizes, int n_in, void* d_out, int out_size,
                              void* d_ws, size_t ws_size, hipStream_t stream) {
  const u16* x0 = (const u16*)d_in[0];
  char* ws = (char*)d_ws;
  size_t off = 0;
  auto alloc = [&](size_t bytes) { char* p = ws + off; off += (bytes + 255) & ~(size_t)255; return p; };
  float2* stats = (float2*)alloc((size_t)S * NPART * 8);
  float* csq = (float*)alloc((size_t)NKC * 16 * 128 * 4);
  float* csg = (float*)alloc((size_t)NKC * 16 * 112 * 4);
  float* csu = (float*)alloc((size_t)NKC * 16 * 112 * 4);
  u16* Pqkv = (u16*)alloc((size_t)16 * 640 * 128 * 2);
  u16* Pg = (u16*)alloc((size_t)16 * 640 * 112 * 2);
  u16* Pu = (u16*)alloc((size_t)16 * 640 * 112 * 2);
  u16* Po = (u16*)alloc((size_t)8 * 640 * 80 * 2);
  u16* Pd = (u16*)alloc((size_t)8 * DF * 80 * 2);
  u16* QKVb = (u16*)alloc((size_t)3 * H * S * HP * 2 + 4096);
  u16* Ob = (u16*)alloc((size_t)S * D * 2);
  u16* xa = (u16*)alloc((size_t)S * D * 2);
  u16* xb = (u16*)alloc((size_t)S * D * 2);
  u16* Hb = (u16*)alloc((size_t)S * DF * 2);

  {
    PrepArgs a{};
    a.x = x0; a.stats = stats;
    a.W[0] = (const u16*)d_in[1]; a.W[1] = (const u16*)d_in[4]; a.W[2] = (const u16*)d_in[5];
    a.W[3] = (const u16*)d_in[2]; a.W[4] = (const u16*)d_in[6];
    a.P[0] = Pqkv; a.P[1] = Pg; a.P[2] = Pu; a.P[3] = Po; a.P[4] = Pd;
    a.csp[0] = csq; a.csp[1] = csg; a.csp[2] = csu;
    prep_kernel<<<160 * 3 + 80 + 216 + 256, 256, 0, stream>>>(a);
  }

#define NLAYERS 2
  const u16* xin = x0;
  for (int layer = 0; layer < NLAYERS; ++layer) {
    u16* x1 = xa;
    u16* x2 = layer == 0 ? xb : (u16*)d_out;
    {
      GemmArgs a{}; a.X = xin; a.W0 = Pqkv; a.stats_in = stats; a.cs0 = csq; a.out0 = QKVb; a.kid = layer * 5 + 0;
      gemm_kernel<EPI_QKV, 128, 8, 120, 1, 640, QKV_NS, 2, 1920, 16, 8><<<256, 512, 0, stream>>>(a);
    }
    attn_kernel<<<256, 512, 0, stream>>>(QKVb, Ob, layer * 5 + 1);
    {
      GemmArgs a{}; a.X = Ob; a.W0 = Po; a.res = xin; a.out0 = x1; a.stats_out = stats; a.kid = layer * 5 + 2;
      gemm_kernel<EPI_RES, 64, 5, 80, 1, 640, RES_NS, 1, 640, 8, 4><<<256, 512, 0, stream>>>(a);
    }
    {
      GemmArgs a{}; a.X = x1; a.W0 = Pg; a.W1 = Pu; a.stats_in = stats; a.cs0 = csg; a.cs1 = csu;
      a.out0 = Hb; a.kid = layer * 5 + 3;
      gemm_kernel<EPI_GEGLU, 128, 7, 108, 2, 640, GU_NS, 2, DF, 16, 4><<<256, 512, 0, stream>>>(a);
    }
    {
      GemmArgs a{}; a.X = Hb; a.W0 = Pd; a.res = x1; a.out0 = x2; a.stats_out = stats; a.kid = layer * 5 + 4;
      gemm_kernel<EPI_RES, 64, 5, 80, 1, DF, RES_NS, 1, 640, 8, 4><<<256, 512, 0, stream>>>(a);
    }
    xin = x2;
  }
}
```

```cpp
#define JBF
#define SPL
#define SPD 36
#define SPS 6
#define MIDC 2
#define XDR 3
#define XP1
#define WOX
#define WOS 16
#define WSD 3
#define WDA 0
#define AIS 2
#define AIB
#define ROT 4
#define JB
#define JP 4
#define TC
#define UNR1
#define OLDPOLL
#define WD0 16
#define PRIO
#define QK8
#define ADR
#define DSY
#define PKP 1
#define PINP
#include <hip/hip_runtime.h>
#include <hip/hip_bf16.h>
#include <stdint.h>

typedef unsigned short u16;
using bf16x8 = __attribute__((ext_vector_type(8))) short;
using bf16x4 = __attribute__((ext_vector_type(4))) short;
using f32x4 = __attribute__((ext_vector_type(4))) float;
using f32x16 = __attribute__((ext_vector_type(16))) float;
using u32x2 = __attribute__((ext_vector_type(2))) unsigned;
using f32x2 = __attribute__((ext_vector_type(2))) float;
using u32x4 = __attribute__((ext_vector_type(4))) unsigned;
typedef short v4i16_t __attribute__((ext_vector_type(4)));
#define LDS_AS __attribute__((address_space(3)))

constexpr int S = 2048, D = 640, H = 32, DH = 20, DF = 1728;
constexpr int NPART = 8;
constexpr int PCOLS = D / NPART;
constexpr int DFP = 16 * 112;
#define XSW 0
#define PSPAD 0
constexpr int PS = S * 16 + PSPAD;
constexpr int HP = 24;

#define TSDECL
#define TS(i)
#define TSFLUSH(kid)

#define STL_MARK(k)
#define LSV(i, k, v)
#define LS(i, k)
template <int V> struct IC { static constexpr int value = V; };
template <int N, class F> __device__ __forceinline__ void static_for(F&& f) {
  if constexpr (N > 0) { static_for<N - 1>(f); f(IC<N - 1>{}); }
}
#define WT 0
#define WT16 1
__device__ __forceinline__ void st8(void* p, u32x2 v) {
  *(u32x2*)p = v;
}
__device__ __forceinline__ void st16(void* base, unsigned off, u32x4 v) {
  __amdgpu_buffer_rsrc_t r = __builtin_amdgcn_make_buffer_rsrc(base, 0, 0x7fffffff, 0x00020000);
  __builtin_amdgcn_raw_buffer_store_b128(v, r, off, 0, 16);
}
__device__ __forceinline__ float bf2f(u16 v) { return __uint_as_float(((unsigned)v) << 16); }
__device__ __forceinline__ unsigned pack2(float lo, float hi) {
  typedef __bf16 bf2_t __attribute__((ext_vector_type(2)));
  return __builtin_bit_cast(unsigned, __builtin_convertvector(f32x2{lo, hi}, bf2_t));
}
__device__ __forceinline__ float dot2(unsigned a, unsigned b, float c) {
  typedef __bf16 bf2_t __attribute__((ext_vector_type(2)));
  return __builtin_amdgcn_fdot2_f32_bf16(__builtin_bit_cast(bf2_t, a), __builtin_bit_cast(bf2_t, b), c, false);
}
__device__ __forceinline__ float sum_g(float x) {
  const auto a = __builtin_amdgcn_permlane16_swap(__float_as_uint(x), __float_as_uint(x), false, false);
  x = __uint_as_float(a[0]) + __uint_as_float(a[1]);
  const auto b = __builtin_amdgcn_permlane32_swap(__float_as_uint(x), __float_as_uint(x), false, false);
  return __uint_as_float(b[0]) + __uint_as_float(b[1]);
}
__device__ __forceinline__ float lo2f(unsigned u) { return __uint_as_float(u << 16); }
__device__ __forceinline__ float hi2f(unsigned u) { return __uint_as_float(u & 0xffff0000u); }

__device__ __forceinline__ void glds16(const void* g, unsigned lb) {
  unsigned sv;
  asm volatile("s_mov_b32 %0, m0\n\ts_mov_b32 m0, %2\n\ts_nop 0\n\tglobal_load_lds_dwordx4 %1, off\n\ts_mov_b32 m0, %0"
               : "=&s"(sv) : "v"(g), "s"(lb) : "memory");
}
__device__ __forceinline__ void glds16c(const void* g, unsigned lb) {
  unsigned sv;
  asm volatile("s_mov_b32 %0, m0\n\ts_mov_b32 m0, %2\n\ts_nop 0\n\tglobal_load_lds_dwordx4 %1, off sc1\n\ts_mov_b32 m0, %0"
               : "=&s"(sv) : "v"(g), "s"(lb) : "memory");
}
__device__ __forceinline__ void glds4(const void* g, unsigned lb) {
  unsigned sv;
  asm volatile("s_mov_b32 %0, m0\n\ts_mov_b32 m0, %2\n\ts_nop 0\n\tglobal_load_lds_dword %1, off\n\ts_mov_b32 m0, %0"
               : "=&s"(sv) : "v"(g), "s"(lb) : "memory");
}
__device__ __forceinline__ void glds4c(const void* g, unsigned lb) {
  unsigned sv;
  asm volatile("s_mov_b32 %0, m0\n\ts_mov_b32 m0, %2\n\ts_nop 0\n\tglobal_load_lds_dword %1, off sc1\n\ts_mov_b32 m0, %0"
               : "=&s"(sv) : "v"(g), "s"(lb) : "memory");
}
#define TOUCH_CODE(sym, endl, ob, lane, junk) do { \
    unsigned long long pc_; int off_, len_; \
    asm volatile("s_getpc_b64 %0\n\ts_mov_b32 %1, " sym "@rel32@lo+4\n\ts_mov_b32 %2, " endl "-" sym : "=s"(pc_), "=s"(off_), "=s"(len_)); \
    const int o_ = ((lane) * 32 + (ob)) * 64; \
    if (o_ < len_) glds4((const char*)(pc_ + (long long)off_) + o_, junk); \
  } while (0)
#define TOUCH_QKV1(ob, lane, junk) TOUCH_CODE("qkv1_kernel", "fused_kernel", ob, lane, junk)
#define TOUCH_FUSED(ob, lane, junk) TOUCH_CODE("fused_kernel", "code_end_mark", ob, lane, junk)
__device__ __forceinline__ void gldsw(const void* g, unsigned lb) { glds16c(g, lb); }
template <bool C> __device__ __forceinline__ void glds(const void* g, unsigned lb) { if constexpr (C) glds16c(g, lb); else glds16(g, lb); }
__device__ __forceinline__ bf16x4 lds_tr(const LDS_AS char* p) {
  return __builtin_amdgcn_ds_read_tr16_b64_v4i16((LDS_AS v4i16_t*)p);
}
template <int N> __device__ __forceinline__ void waitv() {
  static_assert(N >= 0 && N < 64, "");
  asm volatile("s_waitcnt vmcnt(%0)" :: "n"(N) : "memory");
}
__device__ __forceinline__ void barrier_raw() {
  asm volatile("" ::: "memory");
  __builtin_amdgcn_s_waitcnt(0xC07F);
  __builtin_amdgcn_s_barrier();
  asm volatile("" ::: "memory");
}

typedef __attribute__((address_space(1))) unsigned gu32;
constexpr int CNT_STRIDE = 32;
constexpr int NCNT = 11;
constexpr int LDS_TOTAL = 163840, LDS_ARR = LDS_TOTAL - 64;
struct Sig { unsigned* p; int id; int nw; };
__device__ __forceinline__ void do_signal(const Sig& sg, LDS_AS char* lds, int lane) {
  unsigned old = 0;
  if (lane == 0) old = __hip_atomic_fetch_add((LDS_AS unsigned*)(lds + LDS_ARR) + sg.id, 1u, __ATOMIC_RELAXED, __HIP_MEMORY_SCOPE_WORKGROUP);
  old = __builtin_amdgcn_readfirstlane(old);
  if (old == (unsigned)(sg.nw - 1) && sg.p) __hip_atomic_fetch_add((gu32*)sg.p, 1u, __ATOMIC_RELAXED, __HIP_MEMORY_SCOPE_AGENT);
}
#define FSL 1
__device__ __forceinline__ void wait_flags(const unsigned* c) {
  unsigned spins = 0;
#pragma nounroll
  while (__any(__hip_atomic_load((const gu32*)c, __ATOMIC_RELAXED, __HIP_MEMORY_SCOPE_AGENT) == 0u)) {
    __builtin_amdgcn_s_sleep(FSL);
    if (++spins > 400000u) break;
  }
  asm volatile("" ::: "memory");
}
#define WSL 1
#define WDB WD0
#define WDC WD0
#define WDD WD0
#define WSA WSL
#define WSB WSL
#define WSC WSL
template <int D0, int SL>
__device__ __forceinline__ void wait_dep(const unsigned* c, unsigned target, int dep = 0) {
  unsigned spins = 0;
  __builtin_amdgcn_s_sleep(D0);
#pragma nounroll
  while (__builtin_amdgcn_readfirstlane(__hip_atomic_load((const gu32*)c, __ATOMIC_RELAXED, __HIP_MEMORY_SCOPE_AGENT)) < target) {
    __builtin_amdgcn_s_sleep(SL);
    if (++spins > 400000u) break;
  }
  asm volatile("" ::: "memory");
}
#define PSL 3
__device__ __forceinline__ void poll_barrier(const unsigned* c, unsigned target) {
  unsigned p0, p1, p2, p3, z, t, n;
#define PLD(r) "global_load_dword " r ", %4, %7 sc1\n\t"
#define PCK(r) "s_waitcnt vmcnt(3)\n\tv_readfirstlane_b32 %5, " r "\n\ts_cmp_ge_u32 %5, %8\n\ts_cbranch_scc1 2f\n\t" PLD(r)
  asm volatile("v_mov_b32 %4, 0\n\ts_mov_b32 %6, 0x20000\n\t"
               PLD("%0") "s_sleep %9\n\t" PLD("%1") "s_sleep %9\n\t" PLD("%2") "s_sleep %9\n\t" PLD("%3")
               "1:\n\t" PCK("%0") PCK("%1") PCK("%2") PCK("%3")
               "s_sub_u32 %6, %6, 1\n\ts_cmp_lg_u32 %6, 0\n\ts_cbranch_scc1 1b\n"
               "2:\n\ts_waitcnt lgkmcnt(0)\n\ts_barrier\n\ts_waitcnt vmcnt(0)"
               : "=&v"(p0), "=&v"(p1), "=&v"(p2), "=&v"(p3), "=&v"(z), "=&s"(t), "=&s"(n)
               : "s"(c), "s"(target), "n"(PSL) : "memory", "scc");
}
__device__ __forceinline__ u32x2 ld8c(const void* base, unsigned off) {
  __amdgpu_buffer_rsrc_t r = __builtin_amdgcn_make_buffer_rsrc((void*)base, 0, 0x7fffffff, 0x00020000);
  return __builtin_amdgcn_raw_buffer_load_b64(r, off, 0, 16);
}

constexpr int NKG = 20;
constexpr size_t W_CNT = 0, W_CSQ = W_CNT + (size_t)NCNT * 256 * CNT_STRIDE * 4, W_CSG = W_CSQ + (size_t)NKG * 16 * 128 * 4,
                 W_CSU = W_CSG + (size_t)NKG * 16 * 112 * 4, W_PQ = W_CSU + (size_t)NKG * 16 * 112 * 4, W_PG = W_PQ + (size_t)16 * 640 * 128 * 2,
                 W_PU = W_PG + (size_t)16 * 640 * 112 * 2, W_PO = W_PU + (size_t)16 * 640 * 112 * 2, W_PD = W_PO + (size_t)8 * 640 * 80 * 2,
                 W_ACT = W_PD + (size_t)8 * DFP * 80 * 2;
static_assert(W_CSQ % 256 == 0 && W_CSG % 256 == 0 && W_PQ % 256 == 0 && W_PO % 256 == 0 && W_PD % 256 == 0 && W_ACT % 256 == 0, "");
struct PrepArgs {
  const u16* W[5];
  char* ws;
};
typedef const __attribute__((address_space(4))) PrepArgs* KPrep;
__device__ __forceinline__ int qkv_chunk(int nf, int g) {
  const int wn = nf >> 2, ab = nf & 1;
  if (!(nf & 2)) return 15 * wn + 5 * (g >> 1) + 2 * (g & 1) + ab;
  if (g < 2) return 15 * wn + 10 + 2 * g + ab;
  if (ab) return 15 * wn + (g == 2 ? 4 : 14);
  return g == 2 ? 15 * wn + 9 : 31 - wn;
}
__device__ __forceinline__ void st_cs(float* base, unsigned off, f32x4 v) {
  __amdgpu_buffer_rsrc_t r = __builtin_amdgcn_make_buffer_rsrc(base, 0, 0x7fffffff, 0x00020000);
  __builtin_amdgcn_raw_buffer_store_b128(__builtin_bit_cast(u32x4, v), r, off, 0, 16);
}
template <int BNS, int NFT, bool CS>
__device__ __forceinline__ void job_part(u16* P, float* csp, int cb, int kc, int nkc, int u, const LDS_AS char* buf, const LDS_AS char* zp, int lane) {
  constexpr int RS = BNS * 2;
  const int g = lane >> 4, c = lane & 3;
  const LDS_AS char* rp = buf + (u * 32 + 8 * g + ((lane & 15) >> 2)) * RS + (BNS == 120 ? 0 : c * 8);
  const bf16x8 ones = __builtin_bit_cast(bf16x8, u32x4{0x3f803f80u, 0x3f803f80u, 0x3f803f80u, 0x3f803f80u});
  __amdgpu_buffer_rsrc_t rs = __builtin_amdgcn_make_buffer_rsrc(P + ((size_t)cb * (nkc * 2) + kc * 2 + u) * (NFT * 512), 0, NFT * 1024, 0x00020000);
  f32x4 ca = {0.f, 0.f, 0.f, 0.f};
  bf16x8 of[NFT];
#pragma unroll
  for (int nf = 0; nf < NFT; ++nf) {
    const LDS_AS char* p = rp + nf * 32;
    if constexpr (BNS == 120) { const int q = qkv_chunk(nf, c); p = q < 30 ? rp + q * 8 : zp; }
    const bf16x4 t0 = lds_tr(p), t1 = lds_tr(p + 4 * RS);
    of[nf] = bf16x8{t0[0], t0[1], t0[2], t0[3], t1[0], t1[1], t1[2], t1[3]};
  }
#pragma unroll
  for (int nf = 0; nf < NFT; ++nf) {
    const bf16x8 o = of[nf];
    __builtin_amdgcn_raw_buffer_store_b128(__builtin_bit_cast(u32x4, o), rs, nf * 1024 + lane * 16, 0, 16);
    if constexpr (CS) {
      const f32x4 cs = __builtin_amdgcn_mfma_f32_16x16x32_bf16(o, ones, f32x4{0.f, 0.f, 0.f, 0.f}, 0, 0, 0);
      st_cs(csp, (lane & 15) == 0 ? (unsigned)(((kc * 2 + u) * 16 + cb) * (NFT * 16) + nf * 16 + 4 * g) * 4 : 0x80000000u, cs);
    }
  }
}
__device__ __forceinline__ void job_part_gu(char* ws, int cb, int ku, int hw, const LDS_AS char* buf, const LDS_AS char* zp, int lane) {
  constexpr int RS = 224, UP = 32 * RS;
  const int g = lane >> 4, c = lane & 3;
  const LDS_AS char* rr = buf + (8 * g + ((lane & 15) >> 2)) * RS + ((cb * 216) & 15) + hw * 112;
  const LDS_AS char* rp = rr + c * 8;
  const LDS_AS char* r6 = hw && (c & 1) ? zp : rr + 96 + (c & 1) * 8 + (c >> 1) * UP;
  const bf16x8 ones = __builtin_bit_cast(bf16x8, u32x4{0x3f803f80u, 0x3f803f80u, 0x3f803f80u, 0x3f803f80u});
  __amdgpu_buffer_rsrc_t rs = __builtin_amdgcn_make_buffer_rsrc(ws + W_PG + hw * (W_PU - W_PG) + (size_t)(cb * NKG + ku) * (7 * 1024), 0, 7 * 1024, 0x00020000);
  float* csp = (float*)(ws + W_CSG + hw * (W_CSU - W_CSG));
  f32x4 ca = {0.f, 0.f, 0.f, 0.f};
  bf16x8 of[7];
#pragma unroll
  for (int i = 0; i < 7; ++i) {
    const LDS_AS char* p = i < 6 ? rp + (i >> 1) * 32 + (i & 1) * UP : r6;
    const bf16x4 t0 = lds_tr(p), t1 = lds_tr(p + 4 * RS);
    of[i] = bf16x8{t0[0], t0[1], t0[2], t0[3], t1[0], t1[1], t1[2], t1[3]};
  }
#pragma unroll
  for (int i = 0; i < 7; ++i) {
    const bf16x8 o = of[i];
    __builtin_amdgcn_raw_buffer_store_b128(__builtin_bit_cast(u32x4, o), rs, i * 1024 + lane * 16, 0, 16);
    const f32x4 cs = __builtin_amdgcn_mfma_f32_16x16x32_bf16(o, ones, f32x4{0.f, 0.f, 0.f, 0.f}, 0, 0, 0);
    st_cs(csp, (lane & 15) == 0 ? (unsigned)((ku * 16 + cb) * 112 + i * 16 + 4 * g) * 4 : 0x80000000u, cs);
  }
}
constexpr int NJOB = 784, JBUF = 15360;
struct JobL { int r; unsigned vo; const u16 *wa, *wb; unsigned nb; int ppr; };
__device__ __forceinline__ void jobs_prep(JobL& j, KPrep a, int b, int lane, int kh = 0) {
  const int it = lane & 15;
  const bool gu = b >= 80 && b < 400, qk = b >= 624, wo = b < 80;
  const int ppr = gu ? 14 : qk ? 15 : 10, ld = gu ? DF * 2 : qk ? 1920 * 2 : D * 2;
  const int col = ((b & (gu || qk ? 15 : 7)) * (gu ? 216 : qk ? 240 : 160)) & ~15;
  const int wi = gu ? 1 : qk ? 0 : wo ? 3 : 4;
  const u16 *wa = a->W[wi], *wb = a->W[wi + (gu ? 1 : 0)];
  const int kp = (((b - (gu ? 80 : qk ? 624 : wo ? 0 : 400)) >> (gu || qk ? 4 : 3)) << (gu ? 5 : 6)) + (it & (gu ? 7 : 15)) * 4;
  const int ro = b < 64 ? (kp >> 4) * DH + (kp & 15) : (kp - 512) * (DH / 4) + 16;
  const int pc = kp >> 3, h4 = (kp >> 2) & 1;
  const bool pa = b < 400 + 96;
  const int tw = pa ? (pc * 171) >> 9 : (pc - 96) >> 2, q8 = pa ? 4 + pc - tw * 3 : pc & 3;
  const int c = (tw & 1) * 56 + (q8 == 6 ? 40 + 4 * h4 : (q8 & 4) * 4 + 16 * h4 + 4 * q8);
  const int rd = (tw >> 1) * 108 + (c < 108 ? c : 104);
  const int r = gu || qk ? kp : wo ? ro : rd;
  const int lq = (lane >= ppr ? 1 : 0) + (lane >= 2 * ppr ? 1 : 0) + (lane >= 3 * ppr ? 1 : 0);
  j.r = r * ld;
  j.vo = (unsigned)(lq * ld + (lane - lq * ppr) * 16 + col);
  j.nb = b < 80 ? D * D * 2 : b < 624 ? D * DF * 2 : D * 1920 * 2;
  j.wa = wa; j.wb = wb; j.ppr = ppr;
  asm volatile("" : "+v"(j.r), "+v"(j.vo), "+s"(j.nb), "+s"(j.wa), "+s"(j.wb), "+s"(j.ppr));
}
template <int G>
__device__ __forceinline__ void jobs_load(const JobL& j, LDS_AS char* buf, int lane) {
  const __amdgpu_buffer_rsrc_t rs = __builtin_amdgcn_make_buffer_rsrc((void*)(G < 2 ? j.wa : j.wb), 0, j.nb, 0x00020000);
  int so[4];
#pragma unroll
  for (int i = 0; i < 4; ++i) so[i] = __builtin_amdgcn_readlane(j.r, G * 4 + i);
  if (lane < 4 * j.ppr) {
    unsigned sv;
#define JL1(o) "s_nop 0\n\tbuffer_load_dwordx4 %1, %2, " o " offen lds\n\ts_add_u32 m0, m0, %4\n\t"
    asm volatile("s_mov_b32 %0, m0\n\ts_mov_b32 m0, %3\n\ts_nop 3\n\t" JL1("%5") JL1("%6") JL1("%7") JL1("%8") "s_mov_b32 m0, %0"
                 : "=&s"(sv)
                 : "v"(j.vo), "s"(rs), "s"((unsigned)(uintptr_t)buf + G * 256 * j.ppr), "s"(j.ppr * 64), "s"(so[0]), "s"(so[1]), "s"(so[2]), "s"(so[3])
                 : "memory", "scc");
  }
}
__device__ __forceinline__ int jobs_part(char* ws, int b, int u, const LDS_AS char* buf, const LDS_AS char* zp, int lane) {
  asm volatile("" : "+s"(ws), "+s"(b), "+v"(lane));
  if (b >= 80 && b < 400) { job_part_gu(ws, b & 15, (b - 80) >> 4, u, buf, zp, lane); return 8 + (b & 15); }
  if (b >= 624) { job_part<120, 8, true>((u16*)(ws + W_PQ), (float*)(ws + W_CSQ), b & 15, (b - 624) >> 4, 10, u, buf, zp, lane); return 32 + (b & 15); }
  const bool dn = b >= 400;
  job_part<80, 5, false>((u16*)(ws + (dn ? W_PD : W_PO)), nullptr, b & 7, (b - (dn ? 400 : 0)) >> 3, dn ? 28 : 10, u, buf, zp, lane);
  return (dn ? 24 : 0) + (b & 7);
}

enum { EPI_QKV = 0, EPI_RES = 1, EPI_GEGLU = 2 };

struct GemmArgs {
  const u16* X;
  const u16* W0;
  const u16* W1;
  const float2* stats_in;
  const float* cs0;
  const float* cs1;
  const u16* res;
  u16* out0;
  float2* stats_out;
  int rps, rrs;
  int ops, ors;
  int wt;
  int kid;
  unsigned* cnt;
  int dep, sig;
};

constexpr int NLW = 4, NCW = 4;
template <int EPI_, int BM_, int NFT_, int BNS_, int NW_, int K_, int NS_, int WN_, int NCOLS_, int NCB_, int CPX_, int XPM_,
          bool DIRECT_, bool FUSED_, int XO_, int XS1_, int XS2_, int WO_, int WS_, int TAB_, int NSW_ = NS_, int EXO_ = 0>
struct Cfg {
  static constexpr int EPI = EPI_, BM = BM_, NFT = NFT_, BNS = BNS_, NW = NW_, K = K_, NS = NS_, WN = WN_, NCOLS = NCOLS_, NCB = NCB_,
                       CPX = CPX_, XPM = XPM_, XO = XO_, XS1 = XS1_, XS2 = XS2_, WO = WO_, WS = WS_, TAB = TAB_,
                       NSW = NSW_, EXO = EXO_;
  static constexpr bool DIRECT = DIRECT_, FUSED = FUSED_;
};
__device__ __forceinline__ int opaque_bid() { int b = blockIdx.x; asm volatile("" : "+s"(b)); return b; }
template <class C, int NWV, int S0, int S1>
__device__ __forceinline__ void prefetch_w(const u16* W0, const u16* W1, LDS_AS char* lds, int lw, int lane) {
  constexpr int NK = C::K / 64, WB = 2 * C::NFT * 1024, WI = WB / 1024, TW = C::NW * WI, NCG = C::NCB / C::CPX;
  const int bid = opaque_bid();
  const int cb = ((bid & 7) % NCG) * C::CPX + (bid >> 3) % C::CPX;
  const unsigned lb = (unsigned)(uintptr_t)lds + C::WO;
  if constexpr (C::NW == 1 && C::WS == WB) {
    const char* src = (const char*)W0 + (size_t)cb * (NK * WB) + S0 * WB + lw * 1024 + lane * 16;
    constexpr int NP = (S1 - S0) * WI;
#pragma unroll
    for (int it = 0; it < (NP + NWV - 1) / NWV; ++it)
      if ((it + 1) * NWV <= NP || it * NWV + lw < NP) gldsw(src + it * (NWV * 1024), lb + S0 * WB + lw * 1024 + it * (NWV * 1024));
  } else {
#pragma unroll
    for (int it = 0; it < (TW + NWV - 1) / NWV; ++it) {
      const int tw = it * NWV + lw, w = tw >= WI ? 1 : 0;
      if ((it + 1) * NWV <= TW || tw < TW) {
        const char* src = (const char*)(w ? W1 : W0) + (size_t)cb * (NK * WB) + (tw - w * WI) * 1024 + lane * 16;
#pragma unroll
        for (int s = S0; s < S1; ++s) gldsw(src + s * WB, lb + s * C::WS + tw * 1024);
      }
    }
  }
}
template <class C, int PP, int PP1, class Pre>
__device__ __forceinline__ void gemm_phase(const GemmArgs& a, LDS_AS char* lds, Sig& sg, Pre pre) {
  constexpr int EPI = C::EPI, BM = C::BM, NFT = C::NFT, BNS = C::BNS, NW = C::NW, K = C::K, NS = C::NS, WN = C::WN, NCOLS = C::NCOLS,
                NCB = C::NCB, CPX = C::CPX, XPM = C::XPM, NSW = C::NSW;
  constexpr bool DIRECT = C::DIRECT, FUSED = C::FUSED;
  TSDECL; TS(0);
  constexpr int BK = 64, NK = K / BK, NU = K / 32, BNP = NFT * 16;
  constexpr int XB = BM * 128;
  constexpr int WB = 2 * NFT * 1024;
  constexpr int XI = XB / 1024, WI = WB / 1024, T = XI + NW * WI;
  constexpr int LPA = (T + NLW - 1) / NLW, NBIG = T - (LPA - 1) * NLW;
  constexpr int WM = NCW / WN;
  constexpr int MF = BM / WM / 16;
  constexpr int NF = (NW == 2) ? NFT : NFT / WN;
  constexpr int RPX = 32 / CPX, NCG = NCB / CPX;
  auto xoff = [](int s) { return C::XO + (s & 1) * C::XS1 + (s >> 1) * C::XS2; };
  auto woff = [](int s) { return C::WO + s * C::WS; };

  int tid = threadIdx.x;
  if constexpr (FUSED) asm volatile("" : "+v"(tid));
  const int lane = tid & 63;
  const int wid = __builtin_amdgcn_readfirstlane(tid >> 6);
  const int bid = FUSED ? opaque_bid() : (int)blockIdx.x;
  const int xcd = bid & 7, ob = bid >> 3;
  const int rb = (xcd / NCG) * RPX + ob / CPX, cb = (xcd % NCG) * CPX + ob % CPX;
  const int m0 = rb * BM, n0 = cb * BNS;
  const unsigned lds0 = (unsigned)(uintptr_t)lds;
  LDS_AS f32x2* sst = (LDS_AS f32x2*)(lds + C::TAB);
  LDS_AS f32x4* sst4 = (LDS_AS f32x4*)(lds + C::TAB);
  LDS_AS float* scs = (LDS_AS float*)(lds + C::TAB + BM * (EPI == EPI_GEGLU ? 16 : 8));

  if (__builtin_expect(wid >= NCW, 1)) {
    const int lw = (wid - NCW) & 3;
    static_assert(XI % NLW == 0, "");
    const char* src[LPA];
    unsigned strd[LPA];
#pragma unroll
    for (int it = 0; it < LPA; ++it) {
      const int t = it * NLW + lw;
      if (it * NLW < XI) {
        if constexpr (XPM) {
          src[it] = (const char*)a.X + (size_t)((m0 + t * 8 + (lane & 7)) * 16 + ((lane >> 3) ^ (t & XSW)) * PS);
          strd[it] = 8 * PS;
        } else {
          const int row = t * 8 + (lane >> 3);
          src[it] = (const char*)(a.X + (size_t)(m0 + row) * K + ((lane & 7) ^ (row & 7)) * 8);
          strd[it] = BK * 2;
        }
      } else if constexpr (DIRECT) {
        static_assert(!DIRECT || (NFT == 8 && NW == 1 && BNS == 120), "");
        const int row = (t - XI) * 4 + (lane >> 4);
        const int p = (lane & 15) ^ (2 * ((row & 3) | ((row >> 1) & 4)));
        src[it] = (const char*)(a.W0 + (size_t)row * NCOLS + n0 + p * 8);
        strd[it] = BK * NCOLS * 2;
      } else {
        const int tw = t - XI, w = (NW == 2 && tw >= WI) ? 1 : 0;
        src[it] = (const char*)(w ? a.W1 : a.W0) + (size_t)cb * (NK * WB) + (tw - w * WI) * 1024 + lane * 16 + (FUSED ? NSW * WB : 0);
        strd[it] = WB;
      }
    }
    const int rot = !DIRECT ? 0 : ROT == 4 ? (((ob / CPX + ob % CPX) & 3) * 5 + 1) >> 1 : ROT == 2 ? ((ob / CPX + ob % CPX) & 1) * 5
                    : ROT == 5 ? (((ob / CPX + ob % CPX) & 3) * 5) >> 1
                    : ROT == 8 ? ((((ob / CPX) * 5 + 1) >> 1) + ob % CPX) % NK
                    : ROT == 9 ? (((((ob / CPX + ob % CPX) & 3) * 5 + 1) >> 1) + (xcd >> 1) + (xcd & 1)) % NK
                    : ((ob / CPX + ob % CPX) % 5) * 2;
    if constexpr (DIRECT) {
#pragma unroll
      for (int it = 0; it < LPA; ++it) src[it] += strd[it] * (unsigned)rot;
    }
    const bool wld = ((lane & 15) ^ (2 * (((lane >> 4) & 3) | ((lw & 2) << 1)))) != 15;
    constexpr int XPW = XI / NLW;
    int wsl = 0;
    auto issue = [&](int kt, auto xc, auto wc) {
      constexpr bool DX = decltype(xc)::value, DW = decltype(wc)::value;
      const unsigned sbx = lds0 + xoff(kt % NS) + lw * 1024, sbw = lds0 + woff(NSW == NS ? kt % NS : wsl) + lw * 1024;
      if constexpr (NSW != NS && DW) wsl = wsl + 1 == NSW ? 0 : wsl + 1;
      if constexpr (XPM == 2) {
        if (kt == NK - 2) {
#pragma unroll
          for (int it = 0; it < XI / NLW; ++it) {
            src[it] = (const char*)a.X + (size_t)(64 * PS + ((it * NLW + lw) * 2 + (lane >> 5)) * (S * 8) + m0 * 8 + (lane & 31) * 16);
            strd[it] = 16 * S * 8;
          }
        }
      }
#pragma unroll
      for (int it = 0; it < LPA; ++it) {
        if ((it < XPW) ? DX : DW) {
          if (it < LPA - 1 || lw < NBIG) {
            if (it < XPW) glds<FUSED>(src[it], sbx + it * (NLW * 1024));
            else if constexpr (!DIRECT) gldsw(src[it], sbw + (it - XPW) * (NLW * 1024));
            else if (wld) glds16(src[it], sbw + (it - XPW) * (NLW * 1024));
            if constexpr (DIRECT) src[it] += (long long)(kt + rot == NK - 1 ? (1 - NK) * (int)strd[it] : (int)strd[it]);
            else
            src[it] += strd[it];
          }
        }
      }
    };
#define GSS 1
    constexpr bool GS = false;
    unsigned gcnt = lds0 + LDS_ARR + 56, gone = 1u << (8 * lw);
    const unsigned gbase = 0x80808080u - (a.dep ? NK + 1 : 1) * 0x01010101u;
    if constexpr (GS) asm volatile("" : "+v"(gcnt), "+v"(gone));
    auto landed = [&]() __attribute__((always_inline)) {
      asm volatile("s_mov_b64 exec, 1\n\tds_add_u32 %0, %1\n\ts_mov_b64 exec, -1" :: "v"(gcnt), "v"(gone) : "memory");
    };
    auto freed = [&](int j) __attribute__((always_inline)) {
      unsigned x, n, v;
      asm volatile("s_mov_b32 %2, 0x1000000\n"
                   "s_mov_b64 exec, 1\n"
                   "1:\n\tds_read_b32 %0, %3 offset:4\n\ts_sub_u32 %2, %2, 1\n\ts_cbranch_scc1 2f\n\ts_waitcnt lgkmcnt(0)\n\tv_readfirstlane_b32 %1, %0\n\t"
                   "s_add_u32 %1, %1, %4\n\ts_andn2_b32 %1, 0x80808080, %1\n\ts_cbranch_scc0 2f\n\ts_sleep %5\n\ts_branch 1b\n2:\n\ts_mov_b64 exec, -1\n\ts_waitcnt lgkmcnt(0)"
                   : "=&v"(v), "=&s"(x), "=&s"(n) : "v"(gcnt), "s"(gbase - j * 0x01010101u), "n"(GSS) : "memory", "scc");
    };
    TS(3);
    int j0 = 0;
    if constexpr (FUSED) {
      if (sg.nw) { waitv<0>(); do_signal(sg, lds, lane); }
      barrier_raw();
      TS(6);
      constexpr int SRAW = EPI == EPI_GEGLU ? 48 * 1024 : C::TAB + 2048;
      if constexpr (EPI != EPI_RES) {
        static_assert(EPI == EPI_RES || (BM == 128 && NPART == 2 * NLW), "");
#pragma unroll
        for (int i = 0; i < 2; ++i)
          glds16c((const char*)a.stats_in + (size_t)((lw * 2 + i) * S + m0) * 8 + lane * 16, lds0 + SRAW + (lw * 2 + i) * 1024);
      }
#pragma unroll
      for (int s = 0; s < NS; ++s) issue(s, IC<1>{}, IC<0>{});
      waitv<(NS - 1) * XPW>();
      barrier_raw();
      TS(1);
      if constexpr (EPI != EPI_RES) {
        const int row = lw * 32 + (lane & 31);
        f32x2 st[NPART];
#pragma unroll
        for (int i = 0; i < NPART; ++i) st[i] = *(const LDS_AS f32x2*)(lds + SRAW + i * 1024 + row * 8);
        float sm = 0.f;
#pragma unroll
        for (int i = 0; i < NPART; ++i) sm += st[i].x;
        const float mean = sm * (1.f / D);
        float m2 = 0.f;
#pragma unroll
        for (int i = 0; i < NPART; ++i) {
          const float d0 = st[i].x * (1.f / PCOLS) - mean;
          m2 += st[i].y + PCOLS * (d0 * d0);
        }
        const float ve = m2 * (1.f / D) + 1e-5f;
        const float rstd = __builtin_amdgcn_rsqf(ve);
        if constexpr (EPI == EPI_GEGLU) {
          constexpr float k1 = 2.f * 0.7978845608028654f * 1.4426950408889634f, k2 = 0.044715f * k1;
          sst4[row] = f32x4{mean, -k1 * rstd, -k2 * (rstd * rstd * rstd), ve};
        } else {
          sst[row] = f32x2{mean, rstd};
        }
      }
      if constexpr (NSW == NS) {
        auto peel = [&](auto jc) {
          constexpr int j = decltype(jc)::value;
          if constexpr (j < NS - 2) {
            if (lw < NBIG) waitv<(NS - 2 - j) * XPW + j * LPA>(); else waitv<(NS - 2 - j) * XPW + j * (LPA - 1)>();
            if constexpr (GS) { landed(); freed(j); } else barrier_raw();
            issue(j + NS, IC<1>{}, IC<1>{});
          }
        };
        static_assert(NS <= 6, "");
        peel(IC<0>{}); peel(IC<1>{}); peel(IC<2>{}); peel(IC<3>{});
        j0 = NS - 2;
      } else {
        static_assert(NS == 4 && NK - NSW >= NS && NK - NSW + NS - 1 <= NK - NS, "");
        auto iter = [&](int j, auto cwc, auto wc) {
          constexpr int cw = decltype(cwc)::value;
          if (lw < NBIG) waitv<(NS - 2) * XPW + cw * (LPA - XPW)>(); else waitv<(NS - 2) * XPW + cw * (LPA - 1 - XPW)>();
          barrier_raw();
          issue(j + NS, IC<1>{}, wc);
        };
        iter(0, IC<0>{}, IC<1>{}); iter(1, IC<1>{}, IC<1>{}); iter(2, IC<2>{}, IC<1>{});
        for (int j = NS - 1; j < NK - NSW; ++j) iter(j, IC<3>{}, IC<1>{});
        iter(NK - NSW, IC<3>{}, IC<0>{}); iter(NK - NSW + 1, IC<2>{}, IC<0>{}); iter(NK - NSW + 2, IC<1>{}, IC<0>{});
        for (int j = NK - NSW + NS - 1; j < NK - NS; ++j) iter(j, IC<0>{}, IC<0>{});
        j0 = NK - NS;
      }
    } else {
#define Q1I 2
#pragma unroll
    for (int s = 0; s < Q1I; ++s) issue(s, IC<1>{}, IC<1>{});
    if constexpr (DIRECT) {
      static_assert(!DIRECT || NS == 4, "");
      const int row = lane;
      *(LDS_AS u32x4*)(lds + woff(lw) + row * 256 + (15 ^ (2 * ((row & 3) | ((row >> 1) & 4)))) * 16) =
          u32x4{0x3f803f80u, 0x3f803f80u, 0x3f803f80u, 0x3f803f80u};
    }
    TS(6);
    if (lw < NBIG) waitv<(Q1I - 1) * LPA>(); else waitv<(Q1I - 1) * (LPA - 1)>();
    barrier_raw();
    TS(1);
#pragma unroll
    for (int s = Q1I; s < NS; ++s) issue(s, IC<1>{}, IC<1>{});
    }
    constexpr bool TAILPRE = FUSED && EPI == EPI_RES;
    for (int j = j0; j < (TAILPRE ? NK - NS : NK - 1); ++j) {
      if (j + NS <= NK) { if (lw < NBIG) waitv<(NS - 2) * LPA>(); else waitv<(NS - 2) * (LPA - 1)>(); }
      else {
        const int rem = NK - 2 - j;
        if (rem >= 6) { if (lw < NBIG) waitv<(6 * LPA < 64 ? 6 * LPA : 0)>(); else waitv<(6 * LPA < 64 ? 6 * (LPA - 1) : 0)>(); }
        else if (rem == 5) { if (lw < NBIG) waitv<5 * LPA>(); else waitv<5 * (LPA - 1)>(); }
        else if (rem == 4) { if (lw < NBIG) waitv<4 * LPA>(); else waitv<4 * (LPA - 1)>(); }
        else if (rem == 3) { if (lw < NBIG) waitv<3 * LPA>(); else waitv<3 * (LPA - 1)>(); }
        else if (rem == 2) { if (lw < NBIG) waitv<2 * LPA>(); else waitv<2 * (LPA - 1)>(); }
        else if (rem == 1) { if (lw < NBIG) waitv<LPA>(); else waitv<LPA - 1>(); }
        else waitv<0>();
      }
      if constexpr (GS) { landed(); if (j + NS < NK || j == NK - 2) freed(j); } else barrier_raw();
      if (j + NS < NK) issue(j + NS, IC<1>{}, IC<1>{});
    }
    if constexpr (TAILPRE) {
      static_assert(!TAILPRE || NS == 4, "");
      constexpr int TA = NSW == NS ? LPA : XPW, TB = NSW == NS ? LPA - 1 : XPW;
      if (lw < NBIG) waitv<2 * TA>(); else waitv<2 * TB>();
      barrier_raw();
      pre(IC<0>{}, lw, lane);
      if (lw < NBIG) waitv<TA + PP>(); else waitv<TB + PP>();
      barrier_raw();
      pre(IC<1>{}, lw, lane);
      waitv<PP + PP1>();
      barrier_raw();
    }
    TS(2);
    LS(5, LST);
    if constexpr (EPI == EPI_QKV) {
      if constexpr (DIRECT) barrier_raw();
    } else if constexpr (EPI == EPI_GEGLU) {
      pre(IC<0>{}, lw, lane);
      if constexpr (GS) freed(NK - 1); else barrier_raw();
      LS(0, LST);
      pre(IC<1>{}, lw, lane);
    } else {
      barrier_raw();
      barrier_raw();
      pre(IC<2>{}, lw, lane);
    }
  } else {
    const int wm = wid / WN, wn = wid % WN;
    const int wsel = (NW == 2) ? wn : 0;
    const int nfb = (NW == 2) ? 0 : wn * NF;
    const int g = lane >> 4;
    if constexpr (FUSED) {
      if (sg.nw) { waitv<0>(); do_signal(sg, lds, lane); }
      const int c2 = tid - 64;
      const bool csw = c2 >= 0 && c2 < NW * BNP / 2;
      constexpr int NCP = NKG;
      f32x2 v[NCP];
      if (csw) {
        const int w = c2 >= BNP / 2 ? 1 : 0;
        const unsigned co = (unsigned)(w * (NKG * NCB * BNP) + cb * BNP + (c2 - w * (BNP / 2)) * 2) * 4;
#pragma unroll
        for (int i = 0; i < NCP; ++i) v[i] = __builtin_bit_cast(f32x2, ld8c(a.cs0, co + i * (NCB * BNP * 4)));
      }
      if constexpr (EPI == EPI_GEGLU) {
        if (wid == 3 && a.dep == 0) {
          const unsigned* c = a.cnt + (7 * 256 + (lane & 7) * 32 + (bid & 31)) * CNT_STRIDE;
          unsigned spins = 0;
#pragma nounroll
          while (__any(__hip_atomic_load((const gu32*)c, __ATOMIC_RELAXED, __HIP_MEMORY_SCOPE_AGENT) < 32u)) {
            __builtin_amdgcn_s_sleep(2);
            if (++spins > 400000u) break;
          }
        }
      }
      if (wid == 0) wait_dep<(EPI == EPI_GEGLU ? WDB : WDC), (EPI == EPI_GEGLU ? WSB : WSC)>(a.cnt + (a.dep * 256 + rb * NCB + cb) * CNT_STRIDE, 16, a.dep);
      barrier_raw();
      if (csw) {
        f32x2 s2 = {0.f, 0.f};
#pragma unroll
        for (int i = 0; i < NCP; ++i) s2 += v[i];
        *(LDS_AS f32x2*)(scs + c2 * 2) = s2;
      }
    }
    f32x4 acc[NF][MF];
#pragma unroll
    for (int i = 0; i < NF; ++i)
#pragma unroll
      for (int j = 0; j < MF; ++j) acc[i][j] = f32x4{0.f, 0.f, 0.f, 0.f};

    const int xrow = wm * (MF * 16) + (lane & 15);
    int xo[2];
#pragma unroll
    for (int kk = 0; kk < 2; ++kk)
      xo[kk] = XPM ? (xrow >> 3) * 1024 + (((kk * 4 + g) ^ ((lane >> 3) & XSW)) * 128) + (lane & 7) * 16
                   : xrow * 128 + (((kk * 4 + g) ^ (lane & 7)) * 16);
    const int wo = wsel * WB + nfb * 1024 + lane * 16;
    const int jx = DIRECT ? 2 * wn : 0, ix = DIRECT ? 2 * wm : 0;
    int wt[NF];
#pragma unroll
    for (int i = 0; i < NF; ++i) {
      const int r = (lane & 15) >> 2, c = lane & 3;
      const int q = qkv_chunk(nfb + (i ^ ix), c);
      wt[i] = (8 * g + r) * 256 + ((q >> 2) ^ (r | ((g & 1) << 2))) * 32 + (q & 3) * 8;
    }
    f32x4 sq[2] = {f32x4{0.f, 0.f, 0.f, 0.f}, f32x4{0.f, 0.f, 0.f, 0.f}}, sw[2] = {sq[0], sq[0]};
    const bf16x8 ones = __builtin_bit_cast(bf16x8, u32x4{0x3f803f80u, 0x3f803f80u, 0x3f803f80u, 0x3f803f80u});
    bf16x8 xf[2][MF], wf[2][NF];
    auto rdfrags = [&](int u, int slot) {
      const LDS_AS char* sb = lds + xoff((u >> 1) % NS);
      const LDS_AS char* sw = lds + woff((u >> 1) % NS);
      const int kk = u & 1;
      if constexpr (DIRECT) {
#pragma unroll
        for (int j = 0; j < MF; ++j) xf[slot][j] = *(const LDS_AS bf16x8*)(sb + (xo[kk] ^ (jx * 2048) ^ (j & 2) * 2048) + (j & 1) * 2048);
#pragma unroll
        for (int i = 0; i < NF; ++i) {
          const bf16x4 t0 = lds_tr(sw + wt[i] + kk * 8192), t1 = lds_tr(sw + wt[i] + kk * 8192 + 1024);
          wf[slot][i] = bf16x8{t0[0], t0[1], t0[2], t0[3], t1[0], t1[1], t1[2], t1[3]};
        }
      } else {
#pragma unroll
        for (int j = 0; j < MF; ++j) xf[slot][j] = *(const LDS_AS bf16x8*)(sb + (xo[kk] ^ (jx * 2048) ^ (j & 2) * 2048) + (j & 1) * 2048);
#pragma unroll
        for (int i = 0; i < NF; ++i) wf[slot][i] = *(const LDS_AS bf16x8*)(sw + wo + (kk * NFT + i) * 1024);
      }
    };
    auto mfmas = [&](int slot) {
#pragma unroll
      for (int i = 0; i < NF; ++i)
#pragma unroll
        for (int j = 0; j < MF; ++j)
          acc[i][j] = __builtin_amdgcn_mfma_f32_16x16x32_bf16(wf[slot][i], xf[slot][j], acc[i][j], 0, 0, 0);
      if constexpr (DIRECT) {
#pragma unroll
        for (int j = 0; j < 2; ++j) {
          sq[j] = __builtin_amdgcn_mfma_f32_16x16x32_bf16(xf[slot][j], xf[slot][j], sq[j], 0, 0, 0);
          sw[j] = __builtin_amdgcn_mfma_f32_16x16x32_bf16(wf[slot][j], ones, sw[j], 0, 0, 0);
        }
      }
    };
    auto interleave = [&]() {
      constexpr int NR = MF + (DIRECT ? 2 : 1) * NF, NM = MF * NF + (DIRECT ? 4 : 0), NP = NR < NM ? NR : NM;
#pragma unroll
      for (int r = 0; r < NP; ++r) {
        __builtin_amdgcn_sched_group_barrier(0x008, 1, 0);
        __builtin_amdgcn_sched_group_barrier(0x100, 1, 0);
      }
      if constexpr (NM > NP) __builtin_amdgcn_sched_group_barrier(0x008, NM - NP, 0);
      if constexpr (NR > NP) __builtin_amdgcn_sched_group_barrier(0x100, NR - NP, 0);
    };
    float qs[NF];
    unsigned oq[3], one1 = 0, one2 = 0;
    if constexpr (EPI == EPI_QKV) {
      const int h0 = cb * 6 + wn * 3;
      constexpr float QS = 0.22360679774997896f * 1.4426950408889634f;
#pragma unroll
      for (int i = 0; i < NF; ++i) qs[i] = cb * 6 + qkv_chunk(nfb + (i ^ ix), g) / 5 < H ? QS : 1.f;
      const unsigned rowo = (unsigned)(m0 + wm * (MF * 16) + (lane & 15)) * 16;
      const int hB = g < 2 ? h0 + 2 : h0 + 2 * (g & 1);
      oq[0] = (unsigned)(((h0 + (g >> 1)) * 3 + (g & 1)) * (S * 16)) + rowo;
      oq[1] = (unsigned)((hB * 3 + (g < 2 ? g : 2)) * (S * 16)) + rowo;
      oq[2] = g == 2 ? (unsigned)(((h0 + 1) * 3 + 2) * (S * 16)) + rowo : 0x80000000u;
      one1 = hB < H ? 0u : 0x3f80u;
      one2 = h0 + 1 < H ? 0u : 0x3f80u;
      asm volatile("" : "+v"(oq[0]), "+v"(oq[1]), "+v"(oq[2]), "+v"(one1), "+v"(qs[0]), "+v"(qs[1]), "+v"(qs[2]), "+v"(qs[3]));
    }
    barrier_raw();
    TS(1);
    LS(1, LST);
    rdfrags(0, 0);
    constexpr int UNRF = NU / 2;
    constexpr bool MC = EPI == EPI_GEGLU;
    f32x4 cc[NF], rc[MF], rm[2];
    if constexpr (MC) {
      constexpr int MC0 = 4;
      f32x4 cm = {0.f, 0.f, 0.f, 0.f};
      float mn[MF];
      const LDS_AS char* rcp = (const LDS_AS char*)(sst4 + wm * (MF * 16) + (lane & 15));
      const LDS_AS char* ccp = (const LDS_AS char*)(scs + wsel * BNP + 4 * g);
      auto mm = [&](int slot, int i0, int i1, int fi) __attribute__((always_inline)) {
#pragma unroll
        for (int k = i0; k < i1; ++k) {
          const int i = k == 0 ? fi : k <= fi ? k - 1 : k;
#pragma unroll
          for (int j = 0; j < MF; ++j) acc[i][j] = __builtin_amdgcn_mfma_f32_16x16x32_bf16(wf[slot][i], xf[slot][j], acc[i][j], 0, 0, 0);
        }
      };
      static_for<NU - 1>([&](auto uc) __attribute__((always_inline)) {
        constexpr int un = decltype(uc)::value, slot = un & 1, ic = un - MC0;
        constexpr bool c1 = false, c2 = ic >= 0 && ic < NF, rn = ic >= -1 && ic + 1 < NF;
        constexpr int fi = c2 ? ic : 0;
        constexpr int NR = MF + NF + (rn ? 1 : 0) + (ic == -1 ? MF : 0);
        if constexpr (slot) barrier_raw(); else __builtin_amdgcn_s_waitcnt(0xC07F);
        __builtin_amdgcn_sched_barrier(0);
        rdfrags(un + 1, slot ^ 1);
        f32x4 cn = cm;
        if constexpr (rn) cn = *(const LDS_AS f32x4*)(ccp + (ic + 1) * 64);
        if constexpr (ic == -1) {
#pragma unroll
          for (int j = 0; j < MF; ++j) mn[j] = *(const LDS_AS float*)(rcp + j * 256);
        }
        if constexpr (c1) {
#pragma unroll
          for (int j = 0; j < MF; ++j) acc[ic][j] -= mn[j] * cm;
        }
        mm(slot, 0, 4, fi);
#pragma unroll
        for (int r = 0; r < 16; ++r) {
          __builtin_amdgcn_sched_group_barrier(0x008, 1, 0);
          if (c1 && r >= 2 && r < 10) __builtin_amdgcn_sched_group_barrier(0x002, 1, 0);
          if (r < NR) __builtin_amdgcn_sched_group_barrier(0x100, 1, 0);
        }
        __builtin_amdgcn_sched_barrier(0);
        constexpr int K2 = NF;
        if constexpr (un == NU - 2) {
#pragma unroll
          for (int j = 0; j < MF; ++j) rc[j] = *(const LDS_AS f32x4*)(rcp + j * 256);
#pragma unroll
          for (int jj = 0; jj < 2; ++jj) rm[jj] = *(const LDS_AS f32x4*)(rcp + (2 * jj + (lane >> 5)) * 256);
        }
        mm(slot, 4, K2, fi);
        if constexpr (c2) {
#pragma unroll
          for (int j = 0; j < MF; ++j) acc[ic][j] -= mn[j] * cm;
        }
#pragma unroll
        for (int r = 0; r < (K2 - 4) * 4; ++r) {
          __builtin_amdgcn_sched_group_barrier(0x008, 1, 0);
          if (un == NU - 2 && r < 2) __builtin_amdgcn_sched_group_barrier(0x002, 2, 0);
          if (un == NU - 2 && r < 6) __builtin_amdgcn_sched_group_barrier(0x100, 1, 0);
          if (c2 && r >= (K2 < NF ? 0 : 1) && r < (K2 < NF ? 8 : 9)) __builtin_amdgcn_sched_group_barrier(0x002, 1, 0);
        }
        __builtin_amdgcn_sched_barrier(0);
        cm = cn;
      });
    } else {
#pragma unroll UNRF
    for (int u = 0; u < NU - 2; u += 2) {
      __builtin_amdgcn_s_waitcnt(0xC07F);
      __builtin_amdgcn_sched_barrier(0);
      rdfrags(u + 1, 1);
      mfmas(0);
      interleave();
      __builtin_amdgcn_sched_barrier(0);
      barrier_raw();
      __builtin_amdgcn_sched_barrier(0);
      rdfrags(u + 2, 0);
      mfmas(1);
      interleave();
      __builtin_amdgcn_sched_barrier(0);
    }
    __builtin_amdgcn_s_waitcnt(0xC07F);
    __builtin_amdgcn_sched_barrier(0);
    rdfrags(NU - 1, 1);
    mfmas(0);
    interleave();
    __builtin_amdgcn_sched_barrier(0);
    }
    auto consts = [&]() {
      if constexpr (EPI == EPI_GEGLU) {
#pragma unroll
        for (int jj = 0; jj < 2; ++jj) rm[jj] = sst4[wm * (MF * 16) + (2 * jj + (lane >> 5)) * 16 + (lane & 15)];
      }
#pragma unroll
      for (int j = 0; j < MF; ++j) {
        const int ml = wm * (MF * 16) + (j ^ jx) * 16 + (lane & 15);
        if constexpr (EPI == EPI_GEGLU) rc[j] = sst4[ml];
        else { const f32x2 t = sst[ml]; rc[j] = f32x4{t.x, t.y, 0.f, 0.f}; }
      }
#pragma unroll
      for (int i = (EPI == EPI_GEGLU ? 4 : 0); i < NF; ++i) cc[i] = *(const LDS_AS f32x4*)(scs + wsel * BNP + (nfb + (i ^ ix)) * 16 + 4 * g);
    };
    if constexpr (!DIRECT && !MC) { consts(); __builtin_amdgcn_sched_barrier(0); }
    if constexpr (EPI == EPI_GEGLU) {
      barrier_raw();
      LS(0, LST);
      __builtin_amdgcn_sched_barrier(0);
      auto mm = [&](int i, int j) { acc[i][j] = __builtin_amdgcn_mfma_f32_16x16x32_bf16(wf[1][i], xf[1][j], acc[i][j], 0, 0, 0); };
      mm(4, 0); mm(5, 0); mm(4, 1); mm(5, 1);
      mm(6, 0); mm(6, 1); mm(6, 2); mm(6, 3);
      mm(4, 2); mm(5, 2); mm(4, 3); mm(5, 3);
#pragma unroll
      for (int j = 0; j < MF; ++j)
#pragma unroll
        for (int i = 0; i < 4; ++i) mm(i, j);
    } else {
      mfmas(1);
    }
    TS(2);

    if constexpr (EPI == EPI_QKV) {
      if constexpr (DIRECT) {
        LDS_AS float* sf = (LDS_AS float*)sst;
        const int r = lane & 3;
#pragma unroll
        for (int j = 0; j < 2; ++j) {
          if (((lane & 15) >> 2) == g) {
            const float v = r == 0 ? sq[j][0] : r == 1 ? sq[j][1] : r == 2 ? sq[j][2] : sq[j][3];
            sf[(wm * (MF * 16) + (jx + j) * 16 + (lane & 15)) * 2 + 1] = v;
          }
          if ((lane & 15) == 0) *(LDS_AS f32x4*)(scs + (nfb + ix + j) * 16 + 4 * g) = sw[j];
        }
        if (wn == 1 && g == 3) {
#pragma unroll
          for (int j = 0; j < MF; ++j)
            sf[(wm * (MF * 16) + (j ^ jx) * 16 + (lane & 15)) * 2] = wm ? acc[0][j][0] : acc[2][j][0];
        }
        barrier_raw();
        consts();
#pragma unroll
        for (int j = 0; j < MF; ++j) {
          const float mean = rc[j][0] * (1.f / D);
          const float var = fmaxf(rc[j][1] * (1.f / D) - mean * mean, 0.f);
          rc[j][0] = mean; rc[j][1] = __builtin_amdgcn_rsqf(var + 1e-5f);
        }
        __builtin_amdgcn_sched_barrier(0);
      }
      TS(3);
      __amdgpu_buffer_rsrc_t ro = __builtin_amdgcn_make_buffer_rsrc(a.out0, 0, 0x7fffffff, 0x00020000);
      auto fin = [&](auto pc) __attribute__((always_inline)) {
        constexpr int l0 = decltype(pc)::value;
#pragma unroll
        for (int j = 0; j < MF; ++j) {
          u32x2 o[NF];
#pragma unroll
          for (int i = 0; i < NF; ++i) {
            const float sc = rc[j][1] * qs[i];
            const f32x4 c = cc[i];
            const float mu = rc[j][0];
            o[i] = u32x2{pack2(sc * (acc[i][j][0] - mu * c[0]), sc * (acc[i][j][1] - mu * c[1])),
                         pack2(sc * (acc[i][j][2] - mu * c[2]), sc * (acc[i][j][3] - mu * c[3]))};
          }
          const unsigned ro_ = (unsigned)((j ^ jx) * 256);
          __builtin_amdgcn_raw_buffer_store_b128(u32x4{o[l0][0], o[l0][1], o[l0 + 1][0], o[l0 + 1][1]}, ro, oq[0] + ro_, 0, 16);
          const u32x2 A = o[l0 ^ 2], B = o[(l0 ^ 2) + 1];
          __builtin_amdgcn_raw_buffer_store_b128(g < 2 ? u32x4{A[0], A[1], B[0], B[1]} : u32x4{B[0], B[1], one1, 0u}, ro, oq[1] + ro_, 0, 16);
          __builtin_amdgcn_raw_buffer_store_b128(u32x4{A[0], A[1], one2, 0u}, ro, oq[2] + ro_, 0, 16);
        }
      };
      if (ix) fin(IC<2>{}); else fin(IC<0>{});
    } else {
      static_assert(EPI == EPI_GEGLU && MF == 4 && NF == 7, "");
      __amdgpu_buffer_rsrc_t ro = __builtin_amdgcn_make_buffer_rsrc(a.out0, 0, 0x7fffffff, 0x00020000);
      const unsigned obr = (unsigned)((m0 + wm * 64 + (lane & 15)) * 16);
      const unsigned ob = obr + (unsigned)((96 + (cb * 2 + wn) * 4) * PS), oba = obr + (unsigned)(((cb * 2 + wn) * 3) * PS);
      auto unit2 = [&](const f32x4& ga0, const f32x4& up0, const f32x4& r0, const f32x4& ga1, const f32x4& up1, const f32x4& r1,
                       u32x2& o0, u32x2& o1) __attribute__((always_inline)) {
#define FEN4(v) asm volatile("" : "+v"(v[0]), "+v"(v[1]), "+v"(v[2]), "+v"(v[3]))
        const f32x2 ga[4] = {{ga0[0], ga0[1]}, {ga0[2], ga0[3]}, {ga1[0], ga1[1]}, {ga1[2], ga1[3]}};
        const f32x2 up[4] = {{up0[0], up0[1]}, {up0[2], up0[3]}, {up1[0], up1[1]}, {up1[2], up1[3]}};
        f32x2 z[4], gu[4];
#pragma unroll
        for (int e = 0; e < 4; ++e) z[e] = ga[e] * ga[e];
        FEN4(z);
#pragma unroll
        for (int e = 0; e < 4; ++e) z[e] = z[e] * (e < 2 ? r0[2] : r1[2]) + (e < 2 ? r0[1] : r1[1]);
        FEN4(z);
#pragma unroll
        for (int e = 0; e < 4; ++e) z[e] *= ga[e];
        FEN4(z);
#pragma unroll
        for (int e = 0; e < 4; ++e) z[e] = f32x2{__builtin_amdgcn_exp2f(z[e][0]), __builtin_amdgcn_exp2f(z[e][1])};
        FEN4(z);
#pragma unroll
        for (int e = 0; e < 4; ++e) z[e] = z[e] * (e < 2 ? r0[3] : r1[3]) + (e < 2 ? r0[3] : r1[3]);
        FEN4(z);
#pragma unroll
        for (int e = 0; e < 4; ++e) gu[e] = ga[e] * up[e];
#pragma unroll
        for (int e = 0; e < 4; ++e) z[e] = f32x2{__builtin_amdgcn_rcpf(z[e][0]), __builtin_amdgcn_rcpf(z[e][1])};
        FEN4(z);
#pragma unroll
        for (int e = 0; e < 4; ++e) gu[e] *= z[e];
        FEN4(gu);
        o0 = u32x2{pack2(gu[0][0], gu[0][1]), pack2(gu[1][0], gu[1][1])};
        o1 = u32x2{pack2(gu[2][0], gu[2][1]), pack2(gu[3][0], gu[3][1])};
      };
      auto cor = [&](int i, int j) __attribute__((always_inline)) -> f32x4 {
        if constexpr (MC) return acc[i][j]; else return acc[i][j] - rc[j][0] * cc[i];
      };
      u32x2 pe[2], po[2], mx[2], hiq[2];
      unit2(cor(4, 0), cor(5, 0), rc[0], cor(4, 1), cor(5, 1), rc[1], pe[0], po[0]);
      {
        f32x4 ga[2], up[2];
#pragma unroll
        for (int jj = 0; jj < 2; ++jj) {
          const f32x4 t0 = cor(6, 2 * jj), t1 = cor(6, 2 * jj + 1);
#pragma unroll
          for (int r = 0; r < 4; ++r) {
            const auto sw = __builtin_amdgcn_permlane32_swap(__float_as_uint(t0[r]), __float_as_uint(t1[r]), false, false);
            ga[jj][r] = __uint_as_float(sw[0]); up[jj][r] = __uint_as_float(sw[1]);
          }
        }
        unit2(ga[0], up[0], rm[0], ga[1], up[1], rm[1], mx[0], mx[1]);
        if (wn && (g & 1)) { mx[0] = u32x2{0u, 0u}; mx[1] = mx[0]; }
      }
      auto stB = [&](int jj) __attribute__((always_inline)) {
        const auto s0 = __builtin_amdgcn_permlane32_swap(pe[jj][0], po[jj][0], false, false);
        const auto s1 = __builtin_amdgcn_permlane32_swap(pe[jj][1], po[jj][1], false, false);
        __builtin_amdgcn_raw_buffer_store_b128(u32x4{s0[0], s1[0], mx[jj][0], mx[jj][1]}, ro,
                                               oba + (g & 1) * PS + (2 * jj + (g >> 1)) * 256, 0, 16);
        hiq[jj] = u32x2{s0[1], s1[1]};
      };
      stB(0);
      asm volatile("" ::: "memory");
#pragma unroll
      for (int i = 0; i < (MC ? 0 : 4); ++i) cc[i] = *(const LDS_AS f32x4*)(scs + wsel * BNP + i * 16 + 4 * g);
      unit2(cor(4, 2), cor(5, 2), rc[2], cor(4, 3), cor(5, 3), rc[3], pe[1], po[1]);
      stB(1);
      {
        const auto s0 = __builtin_amdgcn_permlane16_swap(hiq[0][0], hiq[1][0], false, false);
        const auto s1 = __builtin_amdgcn_permlane16_swap(hiq[0][1], hiq[1][1], false, false);
        __builtin_amdgcn_raw_buffer_store_b128(u32x4{s0[0], s1[0], s0[1], s1[1]}, ro, oba + 2 * PS + (2 * (g & 1) + (g >> 1)) * 256, 0, 16);
      }
#pragma unroll
      for (int j = 0; j < MF; ++j) {
        u32x2 o0, o1;
        unit2(cor(0, j), cor(1, j), rc[j], cor(2, j), cor(3, j), rc[j], o0, o1);
        __builtin_amdgcn_raw_buffer_store_b128(u32x4{o0[0], o0[1], o1[0], o1[1]}, ro, ob + g * PS + j * 256, 0, 16);
#define SPJ 2
        if (j == SPJ) {
          waitv<SPJ + 1>();
          if (lane < 8)
            __hip_atomic_fetch_add((gu32*)(a.cnt + ((a.sig == 1 ? 9 : 10) * 256 + (rb * 2 + wm) * 8 + lane) * CNT_STRIDE), 1u,
                                   __ATOMIC_RELAXED, __HIP_MEMORY_SCOPE_AGENT);
          asm volatile("" ::: "memory");
        }
      }
      LS(2, LST);
    }
  }
  TS(4);
  if constexpr (FUSED) {
    int idx = -1;
    if constexpr (EPI == EPI_RES) { if (lane < 16) idx = (rb >> 1) * 16 + lane; }
    else if constexpr (EPI == EPI_GEGLU) { if (lane < 16) idx = rb * 16 + lane; }
    else {
      const int gh = cb * 6 + (lane >> 3), qb = lane & 7;
      if (lane < 48 && (gh >= H || qb == (rb >> 1))) idx = (gh & (H - 1)) * 8 + qb;
    }
    sg.p = idx >= 0 ? a.cnt + (a.sig * 256 + idx) * CNT_STRIDE : nullptr;
    sg.id = a.sig;
    sg.nw = wid < NCW ? NCW : 0;
  }
  TS(5);
  TSFLUSH(a.kid);
}

struct RWo {
  static constexpr int NK = 5, XPM = 2, NSX = 5, NSW = 5, NPRE = 3, EXO = 100 * 1024, DEPN = 32, W4H = 122 * 1024, XD = 5;
  static constexpr int woff(int s) { return s == 2 ? 136 * 1024 : (s > 2 ? s - 1 : s) * 20 * 1024; }
  static constexpr int xoff(int s) { return s == 0 ? 72 * 1024 : s == 1 ? 88 * 1024 : s == 2 ? 104 * 1024 : s == 3 ? 40 * 1024 : 56 * 1024; }
#define WOH 2
  static constexpr int pp(int j) { return j == WOH ? 7 : 0; }
  static constexpr int SP = 0, NR = 0, RX = 0, LOOK = 0, CHK = 0;
};
struct RDn {
  static constexpr int NK = DFP / 128, XPM = 1, NSX = 3, NSW = 5, NPRE = 5, EXO = 136 * 1024, DEPN = 16;
  static constexpr int woff(int s) { return (s == 0 ? 136 : s == 1 ? 116 : s == 2 ? 76 : s == 3 ? 96 : 56) * 1024; }
  static constexpr int xoff(int s) { return s * 16 * 1024; }
  static constexpr int W4H = 0;
#define DNH1 11
#define DNH0 12
  static constexpr int pp(int j) { return (j == DNH1 ? 4 : 0) + (j == DNH0 ? 4 : 0); }
  static constexpr int SP = 6;
  static constexpr int NR = 0;
  static constexpr int XD = XDR;
#define XRG 0
#define XLK 1
#define XCK 3
  static constexpr int RX = XRG, LOOK = XLK, CHK = XCK;
};
template <class C> constexpr int xr_req(int j) {
  int m = (j + 1 > C::NSX - 1 ? j + 1 : C::NSX - 1) + C::RX;
  if (j < C::CHK && m > C::SP - 1) m = C::SP - 1;
  return m < C::NK - 1 ? m : C::NK - 1;
}
template <class C> constexpr int xd_req(int j) {
  int m = j + C::XD;
  if (j < C::CHK && m > C::SP - 1) m = C::SP - 1;
  return m < C::NK - 1 ? m : C::NK - 1;
}
template <class C> constexpr int xd_wait(int j) {
  int n = 0;
  for (int s = C::NPRE; s < C::NSW; ++s) n = s <= j + 1 ? 0 : n + 5;
  n += 5 * C::NR;
  for (int i = 0; i < j; ++i) {
    if (i == C::LOOK) n = j >= C::CHK ? 0 : n + 1;
    if (i >= C::NR && i + C::NSW < C::NK) n = i + C::NSW <= j + 1 ? 0 : n + 5;
    n += C::pp(i);
  }
  return n < 63 ? n : 63;
}
template <class C> constexpr int xr_wait(int j) {
  int n = 0;
  for (int s = 0; s <= xr_req<C>(-1); ++s) n = s <= j + 1 ? 0 : n + 4;
  for (int i = 0; i < j; ++i) {
    if (i == C::LOOK) n = j >= C::CHK ? 0 : n + 1;
    for (int s = xr_req<C>(i - 1) + 1; s <= xr_req<C>(i); ++s) n = s <= j + 1 ? 0 : n + 4;
    if (i >= C::NR && i + C::NSW < C::NK) n = i + C::NSW <= j + 1 ? 0 : n + 5;
    n += C::pp(i);
  }
  return n < 63 ? n : 63;
}
template <class C> constexpr int res_behind(int st, int j) {
  int n = 0;
  for (int s = 0; s < C::NSX && s < C::NK; ++s) n = s == st ? 0 : n + 4;
  for (int i = 0; i < j; ++i) {
    if (C::SP && i + C::NSX + 2 == C::SP) n += 1;
    if (i + C::NSX < C::NK) n = i + C::NSX == st ? 0 : n + 4;
    if (i >= C::NR && i + C::NSW < C::NK) n = i + C::NSW == st ? 0 : n + 5;
    n += C::pp(i);
  }
  return n;
}
template <class C, int NWV, int S0, int S1>
__device__ __forceinline__ void prefetch_r(const u16* W, LDS_AS char* lds, int lw, int lane) {
  const int bid = opaque_bid();
  const int cb = (bid & 1) * 4 + ((bid >> 3) & 3);
  const char* src = (const char*)W + (size_t)cb * (C::NK * 20480) + lw * 1024 + lane * 16;
  const unsigned lb = (unsigned)(uintptr_t)lds + lw * 1024;
#pragma unroll
  for (int s = S0; s < S1; ++s)
#pragma unroll
    for (int it = 0; it < (20 + NWV - 1) / NWV; ++it)
      if ((it + 1) * NWV <= 20 || it * NWV + lw < 20) gldsw(src + s * 20480 + it * (NWV * 1024), lb + C::woff(s) + it * (NWV * 1024));
}
template <class C, class Pre>
__device__ __forceinline__ void res_phase(const GemmArgs& a, LDS_AS char* lds, Sig& sg, Pre pre) {
  constexpr int NK = C::NK, XPM = C::XPM, NSX = C::NSX, NSW = C::NSW, NFT = 5, NCB = 8;
  constexpr bool FUSED = true;
  TSDECL; TS(0);
  int tid = threadIdx.x;
  asm volatile("" : "+v"(tid));
  const int lane = tid & 63;
  const int wid = __builtin_amdgcn_readfirstlane(tid >> 6);
  const int bid = opaque_bid();
  const int xcd = bid & 7, ob = bid >> 3;
  const int rb = (xcd >> 1) * 8 + (ob >> 2), cb = (xcd & 1) * 4 + (ob & 3);
  const int m0 = rb * 64;
  const unsigned lds0 = (unsigned)(uintptr_t)lds;
  if (__builtin_expect(wid >= NCW, 1)) {
    const int lw = (wid - NCW) & 3;
    const char* sx[2];
    unsigned stx = 8 * PS;
#pragma unroll
    for (int it = 0; it < 2; ++it) sx[it] = (const char*)a.X + (size_t)((m0 + (it * 4 + lw) * 8 + (lane & 7)) * 16 + (lane >> 3) * PS);
    const char* sw = (const char*)a.W0 + (size_t)cb * (NK * 20480) + C::NPRE * 20480 + lw * 1024 + lane * 16;
    auto issx = [&](auto sc) __attribute__((always_inline)) {
      constexpr int s = decltype(sc)::value;
      if constexpr (XPM == 2 && s == NK - 1) {
#pragma unroll
        for (int it = 0; it < 2; ++it)
          sx[it] = (const char*)a.X + (size_t)(64 * PS + ((it * 4 + lw) * 2 + (lane >> 5)) * (S * 8) + m0 * 8 + (lane & 31) * 16);
        stx = 16 * S * 8;
      }
#pragma unroll
      for (int hk = 0; hk < 2; ++hk)
#pragma unroll
        for (int it = 0; it < 2; ++it) {
          glds16c(sx[it], lds0 + C::xoff(s % NSX) + hk * 8192 + it * 4096 + lw * 1024);
          sx[it] += stx;
        }
    };
    constexpr int NR = C::NR;
    u32x4 wr[NR > 0 ? NR : 1][5];
    auto issw = [&](auto sc) __attribute__((always_inline)) {
      constexpr int s = decltype(sc)::value;
      if constexpr (NR > 0 && s >= NSW && s < NSW + NR) {
#pragma unroll
        for (int it = 0; it < 5; ++it) *(LDS_AS u32x4*)(lds + C::woff(s % NSW) + it * 4096 + lw * 1024 + lane * 16) = wr[s - NSW][it];
      } else {
#pragma unroll
        for (int it = 0; it < 5; ++it) gldsw(sw + it * 4096, lds0 + C::woff(s % NSW) + it * 4096 + lw * 1024);
        sw += 20480;
      }
    };
    auto ldwr = [&]() __attribute__((always_inline)) {
    if constexpr (NR > 0) {
      static_assert(NR == 0 || (C::SP && NSW + NR <= NK), "");
      __amdgpu_buffer_rsrc_t rw =
          __builtin_amdgcn_make_buffer_rsrc((char*)a.W0 + (size_t)cb * (NK * 20480) + NSW * 20480, 0, 0x7fffffff, 0x00020000);
#pragma unroll
      for (int s = 0; s < NR; ++s)
#pragma unroll
        for (int it = 0; it < 5; ++it)
          wr[s][it] = __builtin_amdgcn_raw_buffer_load_b128(rw, lw * 1024 + lane * 16, s * 20480 + it * 4096, 16);
      sw += NR * 20480;
      asm volatile("" ::: "memory");
    }
    };
    ldwr();
    TS(3);
    if (sg.nw) { waitv<0>(); do_signal(sg, lds, lane); }
    const unsigned* c2 = a.cnt + (a.dep * 256 + rb * NCB + cb) * CNT_STRIDE;
    if constexpr (C::SP) {
#define SPW 0
      const unsigned* c1 = a.cnt + ((a.dep == 1 ? 9 : 10) * 256 + rb * NCB + cb) * CNT_STRIDE;
      unsigned spins = 0;
      asm volatile("" : "+v"(sx[0]), "+v"(sx[1]), "+v"(sw), "+s"(c2));
      if (lw == 0)
      {
      __builtin_amdgcn_s_sleep(SPD);
      LS(1, LST + 1);
#pragma nounroll
      while (__builtin_amdgcn_readfirstlane(__hip_atomic_load((const gu32*)c1, __ATOMIC_RELAXED, __HIP_MEMORY_SCOPE_AGENT)) < 32u) {
        __builtin_amdgcn_s_sleep(SPS);
        if (++spins > 400000u) break;
      }
      }
      asm volatile("" ::: "memory");
    } else {
      if constexpr (C::XD >= NK) {
        issw(IC<3>{});
#pragma unroll
        for (int it = 0; it < 5; ++it)
          gldsw(sw + it * 4096, lds0 + (it * 4 + lw < 10 ? C::woff(4) : C::W4H - 10240) + it * 4096 + lw * 1024);
        waitv<0>();
      }
      barrier_raw();
    }
    TS(6);
    LS(2, LST + 1);
    if constexpr (C::XD == 0) static_for<(NSX < NK ? NSX : NK)>(issx);
    constexpr int RX = C::RX;
    if constexpr (C::XD >= NK) {
      __builtin_amdgcn_s_sleep(WOS);
      pre(IC<WOH>{}, lw, lane);
    } else if constexpr (C::XD > 0) {
      waitv<xd_wait<C>(-1)>();
      barrier_raw();
      static_for<NK - 1>([&](auto jc) __attribute__((always_inline)) {
        constexpr int j = decltype(jc)::value;
        waitv<xd_wait<C>(j)>();
        if constexpr (j == C::CHK) {
          unsigned v = *(const LDS_AS unsigned*)(lds + LDS_ARR + 32 + lw * 4), spins = 0;
#pragma nounroll
          while (__builtin_expect(__builtin_amdgcn_readfirstlane(v) < (unsigned)C::DEPN, 0)) {
            asm volatile("global_load_dword %0, %1, off sc1\n\ts_waitcnt vmcnt(0)" : "=v"(v) : "v"(c2) : "memory");
            if (++spins > 400000u) break;
          }
          asm volatile("" ::: "memory");
          LSV(6, LST + 1, spins);
        }
        barrier_raw();
        if constexpr (j == 3) LS(4, LST + 1);
        if constexpr (j == C::LOOK) { if (lane == 0) glds4c(c2, lds0 + LDS_ARR + 32 + lw * 4); }
        if constexpr (j + NSW < NK) issw(IC<j + NSW>{});
        if constexpr (C::pp(j) > 0) pre(jc, lw, lane);
      });
    } else if constexpr (RX > 0) {
      static_assert(RX == 0 || (C::SP > NSX && XPM == 1 && C::LOOK < C::CHK && C::CHK + 2 <= C::SP), "");
      u32x4 xr[RX][4];
      unsigned xa = lds0 + lw * 1024 + lane * 16;
      asm volatile("" : "+v"(xa));
      auto reqx = [&](auto lc, auto hc) __attribute__((always_inline)) {
        constexpr int lo = decltype(lc)::value, hi = decltype(hc)::value;
        static_for<(hi > lo ? hi - lo : 0)>([&](auto kc) __attribute__((always_inline)) {
          constexpr int s = lo + 1 + decltype(kc)::value;
#pragma unroll
          for (int q = 0; q < 4; ++q) {
            asm volatile("global_load_dwordx4 %0, %1, off sc1" : "=v"(xr[s % RX][q]) : "v"(sx[q & 1]) : "memory");
            sx[q & 1] += stx;
          }
        });
      };
      reqx(IC<NSX - 1>{}, IC<xr_req<C>(-1)>{});
      waitv<xr_wait<C>(-1)>();
      LS(3, LST + 1);
      barrier_raw();
      static_for<NK - 1>([&](auto jc) __attribute__((always_inline)) {
        constexpr int j = decltype(jc)::value;
        waitv<xr_wait<C>(j)>();
        if constexpr (j + 1 >= NSX) {
#pragma unroll
          for (int q = 0; q < 4; ++q)
            asm volatile("ds_write_b128 %0, %1 offset:%2" :: "v"(xa), "v"(xr[(j + 1) % RX][q]),
                         "n"(C::xoff((j + 1) % NSX) + (q >> 1) * 8192 + (q & 1) * 4096) : "memory");
        }
        barrier_raw();
        if constexpr (j == C::LOOK) { if (lane == 0) glds4c(c2, lds0 + LDS_ARR + 32 + lw * 4); }
        if constexpr (j == C::CHK) {
          unsigned v = *(const LDS_AS unsigned*)(lds + LDS_ARR + 32 + lw * 4), spins = 0;
#pragma nounroll
          while (__builtin_expect(__builtin_amdgcn_readfirstlane(v) < (unsigned)C::DEPN, 0)) {
            asm volatile("global_load_dword %0, %1, off sc1\n\ts_waitcnt vmcnt(0)" : "=v"(v) : "v"(c2) : "memory");
            if (++spins > 400000u) break;
          }
          asm volatile("" ::: "memory");
          LSV(6, LST + 1, spins);
        }
        if constexpr (j == 3) LS(4, LST + 1);
        reqx(IC<xr_req<C>(j - 1)>{}, IC<xr_req<C>(j)>{});
        if constexpr (j + NSW < NK) issw(IC<j + NSW>{});
        if constexpr (C::pp(j) > 0) pre(jc, lw, lane);
      });
    } else {
    waitv<res_behind<C>(0, -1)>();
    LS(3, LST + 1);
    barrier_raw();
    TS(1);
    static_for<NK - 1>([&](auto jc) __attribute__((always_inline)) {
      constexpr int j = decltype(jc)::value;
      waitv<res_behind<C>(j + 1, j)>();
      barrier_raw();
      if constexpr (C::SP && j + NSX + 2 == C::SP) { if (lane == 0) glds4c(c2, lds0 + LDS_ARR + 32 + lw * 4); }
      if constexpr (C::SP && j + NSX == C::SP) {
        unsigned v = *(const LDS_AS unsigned*)(lds + LDS_ARR + 32 + lw * 4), spins = 0;
#pragma nounroll
        while (__builtin_expect(__builtin_amdgcn_readfirstlane(v) < (unsigned)C::DEPN, 0)) {
          asm volatile("global_load_dword %0, %1, off sc1\n\ts_waitcnt vmcnt(0)" : "=v"(v) : "v"(c2) : "memory");
          if (++spins > 400000u) break;
        }
        asm volatile("" ::: "memory");
        LSV(6, LST + 1, spins);
      }
      if constexpr (j == 3) LS(4, LST + 1);
      if constexpr (j + NSX < NK) issx(IC<j + NSX>{});
      if constexpr (j + NSW < NK) issw(IC<j + NSW>{});
      if constexpr (C::pp(j) > 0) pre(jc, lw, lane);
    });
    }
    TS(2);
    barrier_raw();
    barrier_raw();
    pre(IC<100>{}, lw, lane);
  } else {
    const int wm2 = wid & 1, kg = (wid >> 1) & 1, g = lane >> 4;
    const int mrow = m0 + (wm2 * 2 + kg) * 16 + (lane & 15);
    if (sg.nw) { waitv<0>(); LS(3, LST + 1); do_signal(sg, lds, lane); }
    if (a.dep < 0) barrier_raw();
    f32x4 acc[NFT][2];
    {
      u32x2 resr[NFT];
#pragma unroll
      for (int i = 0; i < NFT; ++i)
        resr[i] = ld8c(a.res, (unsigned)(mrow * a.rrs + (cb * (2 * NFT) + 2 * i + (g >> 1)) * a.rps + (g & 1) * 8));
      if (!C::SP && C::XD < NK && a.dep >= 0) {
        if (wid == 0) wait_dep<WDA, WSA>(a.cnt + (a.dep * 256 + rb * NCB + cb) * CNT_STRIDE, C::DEPN, a.dep);
        barrier_raw();
      }
#pragma unroll
      for (int i = 0; i < NFT; ++i) {
        acc[i][0] = f32x4{lo2f(resr[i][0]), hi2f(resr[i][0]), lo2f(resr[i][1]), hi2f(resr[i][1])};
        acc[i][1] = f32x4{0.f, 0.f, 0.f, 0.f};
        asm volatile("" : "+v"(acc[i][0]), "+v"(acc[i][1]));
      }
    }
    const int xo = (wm2 * 4 + kg * 2 + ((lane >> 3) & 1)) * 1024 + (kg * 4 + g) * 128 + (lane & 7) * 16;
    const int xo1 = xo ^ 2048;
    const int wo = kg * (NFT * 1024) + lane * 16;
    const int xc = (kg * 8 + 2 * g) * 512 + (wm2 * 32 + kg * 16 + (lane & 15)) * 8;
    bf16x8 xf[2][2][2], wf[2][2][NFT];
    auto rd2 = [&](const LDS_AS char* p) __attribute__((always_inline)) -> bf16x8 {
      const u32x2 l = *(const LDS_AS u32x2*)p, h = *(const LDS_AS u32x2*)(p + 512);
      return __builtin_bit_cast(bf16x8, u32x4{l[0], l[1], h[0], h[1]});
    };
    auto rd = [&](auto sc) __attribute__((always_inline)) {
      constexpr int s = decltype(sc)::value, b = s & 1;
      constexpr bool cc = XPM == 2 && s == NK - 1;
#pragma unroll
      for (int hk = 0; hk < 2; ++hk) {
        const LDS_AS char* sb = lds + C::xoff(s % NSX) + hk * 8192;
        const LDS_AS char* sw = lds + C::woff(s % NSW) + hk * 10240;
        if constexpr (cc) xf[b][hk][0] = rd2(sb + xc); else xf[b][hk][0] = *(const LDS_AS bf16x8*)(sb + xo);
#pragma unroll
        for (int i = 0; i < NFT; ++i) wf[b][hk][i] = *(const LDS_AS bf16x8*)(sw + wo + i * 1024);
        if constexpr (cc) xf[b][hk][1] = rd2(sb + (xc ^ 128)); else xf[b][hk][1] = *(const LDS_AS bf16x8*)(sb + xo1);
      }
    };
    auto mf = [&](auto bc) __attribute__((always_inline)) {
      constexpr int b = decltype(bc)::value;
#pragma unroll
      for (int hk = 0; hk < 2; ++hk)
#pragma unroll
        for (int j = 0; j < 2; ++j)
#pragma unroll
          for (int i = 0; i < NFT; ++i)
            acc[i][j] = __builtin_amdgcn_mfma_f32_16x16x32_bf16(wf[b][hk][i], xf[b][hk][j], acc[i][j], 0, 0, 0);
    };
    if constexpr (C::XD >= NK) {
      __amdgpu_buffer_rsrc_t rx = __builtin_amdgcn_make_buffer_rsrc((void*)a.X, 0, 0x7fffffff, 0x00020000);
      unsigned vx[2], vc[2];
      vx[0] = (unsigned)((kg * 4 + g) * PS + mrow * 16);
      vx[1] = vx[0] ^ 256u;
      vc[0] = (unsigned)((kg * 8 + 2 * g) * (S * 8) + mrow * 8);
      vc[1] = vc[0] ^ 128u;
      const LDS_AS char* wb[3] = {lds + wo, lds + wo + 40 * 1024, lds + wo + C::W4H};
      asm volatile("" : "+v"(vx[0]), "+v"(vx[1]), "+v"(vc[0]), "+v"(vc[1]), "+v"(wb[0]), "+v"(wb[1]), "+v"(wb[2]));
      bf16x8 xg[NK][2][2];
      auto rdw = [&](auto sc) __attribute__((always_inline)) {
        constexpr int s = decltype(sc)::value;
#pragma unroll
        for (int hk = 0; hk < 2; ++hk)
#pragma unroll
          for (int i = 0; i < NFT; ++i) {
            const LDS_AS char* p = s < 2 ? wb[0] + C::woff(s) + hk * 10240 : s == 2 ? wb[2] + (C::woff(2) - C::W4H) + hk * 10240
                                 : s == 4 && hk ? wb[2] : wb[1] + (C::woff(s) - 40 * 1024) + hk * 10240;
            wf[s & 1][hk][i] = *(const LDS_AS bf16x8*)(p + i * 1024);
          }
      };
      auto mg = [&](auto sc) __attribute__((always_inline)) {
        constexpr int s = decltype(sc)::value;
#pragma unroll
        for (int hk = 0; hk < 2; ++hk)
#pragma unroll
          for (int j = 0; j < 2; ++j)
#pragma unroll
            for (int i = 0; i < NFT; ++i)
              acc[i][j] = __builtin_amdgcn_mfma_f32_16x16x32_bf16(wf[s & 1][hk][i], xg[s][hk][j], acc[i][j], 0, 0, 0);
      };
      if (wid == 0) wait_dep<WDA, WSA>(a.cnt + (a.dep * 256 + rb * NCB + cb) * CNT_STRIDE, C::DEPN, a.dep);
      barrier_raw();
      TS(1);
      __builtin_amdgcn_sched_barrier(0);
#pragma unroll
      for (int s = 0; s < NK; ++s)
#pragma unroll
        for (int q = 0; q < 4; ++q) {
          if (s < NK - 1) {
            xg[s][q >> 1][q & 1] = __builtin_bit_cast(bf16x8, __builtin_amdgcn_raw_buffer_load_b128(rx, vx[q & 1], (s * 16 + (q >> 1) * 8) * PS, 16));
          } else {
            const u32x2 l = __builtin_amdgcn_raw_buffer_load_b64(rx, vc[q & 1], 64 * PS + (q >> 1) * (16 * S * 8), 16);
            const u32x2 h = __builtin_amdgcn_raw_buffer_load_b64(rx, vc[q & 1], 64 * PS + (q >> 1) * (16 * S * 8) + S * 8, 16);
            xg[s][q >> 1][q & 1] = __builtin_bit_cast(bf16x8, u32x4{l[0], l[1], h[0], h[1]});
          }
          __builtin_amdgcn_sched_barrier(0);
        }
      rdw(IC<0>{});
      __builtin_amdgcn_sched_barrier(0);
      static_for<NK - 1>([&](auto jc) __attribute__((always_inline)) {
        constexpr int j = decltype(jc)::value;
        rdw(IC<j + 1>{});
        mg(jc);
#pragma unroll
        for (int r = 0; r < 4 * NFT; ++r) {
          if (r < 2 * NFT) __builtin_amdgcn_sched_group_barrier(0x100, 1, 0);
          __builtin_amdgcn_sched_group_barrier(0x008, 1, 0);
        }
        __builtin_amdgcn_sched_barrier(0);
      });
      mg(IC<NK - 1>{});
    } else if constexpr (C::XD > 0) {
      static_assert(C::XD == 0 || C::XD >= NK || (XPM == 1 && PS % 512 == 0 && C::LOOK < C::CHK && C::XD <= C::SP), "");
      __amdgpu_buffer_rsrc_t rx = __builtin_amdgcn_make_buffer_rsrc((void*)a.X, 0, 0x7fffffff, 0x00020000);
      unsigned vx[2];
      vx[0] = (unsigned)((kg * 4 + g) * PS + mrow * 16);
      vx[1] = vx[0] ^ 256u;
      asm volatile("" : "+v"(vx[0]), "+v"(vx[1]));
      bf16x8 xg[NK][2][2];
      const LDS_AS char* wb[4];
#pragma unroll
      for (int i = 0; i < 4; ++i) { wb[i] = lds + wo + i * (40 * 1024); asm volatile("" : "+v"(wb[i])); }
      auto ldx = [&](auto lc, auto hc) __attribute__((always_inline)) {
        constexpr int lo = decltype(lc)::value, hi = decltype(hc)::value;
#pragma unroll
        for (int s = lo + 1; s <= hi; ++s)
#pragma unroll
          for (int q = 0; q < 4; ++q)
            xg[s][q >> 1][q & 1] = __builtin_bit_cast(bf16x8, __builtin_amdgcn_raw_buffer_load_b128(rx, vx[q & 1], (s * 16 + (q >> 1) * 8) * PS, 16));
      };
      auto rdw = [&](auto sc) __attribute__((always_inline)) {
        constexpr int s = decltype(sc)::value;
#pragma unroll
        for (int hk = 0; hk < 2; ++hk)
#pragma unroll
          for (int i = 0; i < NFT; ++i) {
            constexpr int o = C::woff(s % NSW);
            wf[s & 1][hk][i] = *(const LDS_AS bf16x8*)(wb[o / (40 * 1024)] + o % (40 * 1024) + hk * 10240 + i * 1024);
          }
      };
      auto mg = [&](auto sc) __attribute__((always_inline)) {
        constexpr int s = decltype(sc)::value;
#pragma unroll
        for (int hk = 0; hk < 2; ++hk)
#pragma unroll
          for (int j = 0; j < 2; ++j)
#pragma unroll
            for (int i = 0; i < NFT; ++i)
              acc[i][j] = __builtin_amdgcn_mfma_f32_16x16x32_bf16(wf[s & 1][hk][i], xg[s][hk][j], acc[i][j], 0, 0, 0);
      };
      barrier_raw();
      TS(1);
      LS(4, LST + 1);
      static_for<xd_req<C>(-1) + 1>([&](auto sc) __attribute__((always_inline)) {
        ldx(IC<decltype(sc)::value - 1>{}, sc);
        __builtin_amdgcn_sched_barrier(0);
      });
      rdw(IC<0>{});
      static_for<NK - 1>([&](auto jc) __attribute__((always_inline)) {
        constexpr int j = decltype(jc)::value, NV = (xd_req<C>(j) - xd_req<C>(j - 1)) * 4;
        barrier_raw();
        __builtin_amdgcn_sched_barrier(0);
        ldx(IC<xd_req<C>(j - 1)>{}, IC<xd_req<C>(j)>{});
        rdw(IC<j + 1>{});
        mg(jc);
#pragma unroll
        for (int r = 0; r < 4 * NFT; ++r) {
          __builtin_amdgcn_sched_group_barrier(0x008, 1, 0);
          if (r < 2 * NFT) __builtin_amdgcn_sched_group_barrier(0x100, 1, 0);
          else if (r - 2 * NFT < NV) __builtin_amdgcn_sched_group_barrier(0x020, NV > 8 ? 2 : 1, 0);
        }
        __builtin_amdgcn_sched_barrier(0);
      });
      mg(IC<NK - 1>{});
    } else {
    barrier_raw();
    TS(1);
    LS(4, LST + 1);
    rd(IC<0>{});
    static_for<NK - 1>([&](auto jc) __attribute__((always_inline)) {
      constexpr int j = decltype(jc)::value, NRD = 2 * (NFT + 2) + (XPM == 2 && j + 1 == NK - 1 ? 4 : 0);
      barrier_raw();
      __builtin_amdgcn_sched_barrier(0);
      rd(IC<j + 1>{});
      mf(IC<(j & 1)>{});
#pragma unroll
      for (int r = 0; r < NRD; ++r) {
        __builtin_amdgcn_sched_group_barrier(0x008, 1, 0);
        __builtin_amdgcn_sched_group_barrier(0x100, 1, 0);
      }
      __builtin_amdgcn_sched_group_barrier(0x008, 4 * NFT - NRD, 0);
      __builtin_amdgcn_sched_barrier(0);
    });
    mf(IC<((NK - 1) & 1)>{});
    }
    TS(2);
    LS(5, LST + 1);
    {
      LDS_AS f32x4* ex = (LDS_AS f32x4*)(lds + C::EXO);
#pragma unroll
      for (int i = 0; i < NFT; ++i) ex[(wid * NFT + i) * 64 + lane] = acc[i][1];
      barrier_raw();
      TS(3);
      u32x2 o[NFT + 1];
      f32x4 tex[NFT];
#pragma unroll
      for (int i = 0; i < NFT; ++i) tex[i] = ex[((wid ^ 2) * NFT + i) * 64 + lane];
#pragma unroll
      for (int i = 0; i < NFT; ++i) {
        const f32x4 t = tex[i] + acc[i][0];
        o[i] = u32x2{pack2(t[0], t[1]), pack2(t[2], t[3])};
      }
      o[NFT] = o[NFT - 1];
      float s2[2] = {0.f, 0.f}, q2[2] = {0.f, 0.f};
#pragma unroll
      for (int i = 0; i < NFT; ++i)
#pragma unroll
        for (int r = 0; r < 2; ++r) { s2[r] = dot2(o[i][r], 0x3f803f80u, s2[r]); q2[r] = dot2(o[i][r], o[i][r], q2[r]); }
      {
        static_assert(NFT == 5, "");
        __amdgpu_buffer_rsrc_t ro = __builtin_amdgcn_make_buffer_rsrc(a.out0, 0, 0x7fffffff, 0x00020000);
        const unsigned ob = (unsigned)(mrow * a.ors + (cb * (2 * NFT) + 2 * (g & 1) + (g >> 1)) * a.ops);
        u32x4 v[3];
#pragma unroll
        for (int i = 0; i < NFT; i += 2) {
          const auto s0 = __builtin_amdgcn_permlane16_swap(o[i][0], o[i + 1][0], false, false);
          const auto s1 = __builtin_amdgcn_permlane16_swap(o[i][1], o[i + 1][1], false, false);
          v[i >> 1] = u32x4{s0[0], s1[0], s0[1], s1[1]};
        }
        const unsigned o2 = (g & 1) ? 0x80000000u : ob + 8 * a.ops;
        if (a.wt) {
          __builtin_amdgcn_raw_buffer_store_b128(v[0], ro, ob, 0, 16);
          __builtin_amdgcn_raw_buffer_store_b128(v[1], ro, ob + 4 * a.ops, 0, 16);
          __builtin_amdgcn_raw_buffer_store_b128(v[2], ro, o2, 0, 16);
        } else {
          __builtin_amdgcn_raw_buffer_store_b128(v[0], ro, ob, 0, 0);
          __builtin_amdgcn_raw_buffer_store_b128(v[1], ro, ob + 4 * a.ops, 0, 0);
          __builtin_amdgcn_raw_buffer_store_b128(v[2], ro, o2, 0, 0);
        }
      }
      const float sm = sum_g(s2[0] + s2[1]);
      const float q = fmaxf(sum_g(q2[0] + q2[1]) - sm * sm * (1.f / PCOLS), 0.f);
      if (lane < 16) {
        __amdgpu_buffer_rsrc_t rt = __builtin_amdgcn_make_buffer_rsrc(a.stats_out, 0, 0x7fffffff, 0x00020000);
        __builtin_amdgcn_raw_buffer_store_b64(u32x2{__float_as_uint(sm), __float_as_uint(q)}, rt, (unsigned)(cb * S + mrow) * 8, 0, 16);
      }
      barrier_raw();
    }
  }
  TS(4);
  {
    const int idx = (rb >> 1) * 16 + lane;
    sg.p = lane < 16 ? a.cnt + (a.sig * 256 + idx) * CNT_STRIDE : nullptr;
    sg.id = a.sig;
    sg.nw = wid < NCW ? NCW : 0;
  }
  TS(5);
  TSFLUSH(a.kid);
}

constexpr int KB = 1024;
using CfgQ1 = Cfg<EPI_QKV, 128, 8, 120, 1, 640, 4, 2, 1920, 16, 8, 0, true, false, 0, 32 * KB, 64 * KB, 16 * KB, 32 * KB, 128 * KB>;
using CfgWo = Cfg<EPI_RES, 64, 5, 80, 1, 640, 4, 1, 640, 8, 4, 2, false, true, 40 * KB, 8 * KB, 16 * KB, 0, 10 * KB, 0, 4, 20 * KB>;
#define GUC 4
#define Q2C 8
using CfgGU = Cfg<EPI_GEGLU, 128, 7, 108, 2, 640, 3, 2, DF, 16, GUC, 1, false, true, 0, 16 * KB, 32 * KB, 72 * KB, 28 * KB, 156 * KB>;
constexpr int DNW = 12;
using CfgDn = Cfg<EPI_RES, 64, 5, 80, 1, DFP, 4, 1, 640, 8, 4, 1, false, true, 0, 8 * KB, 16 * KB, 146 * KB, -10 * KB, 0, DNW, 136 * KB>;
using CfgQ2 = Cfg<EPI_QKV, 128, 8, 120, 1, 640, 4, 2, 1920, 16, Q2C, 1, false, true, 0, 16 * KB, 96 * KB, 32 * KB, 16 * KB, 128 * KB>;
extern "C" __global__ __launch_bounds__(512) void qkv1_kernel(GemmArgs a) {
  __shared__ __attribute__((aligned(16))) char smem[128 * KB + 128 * 8 + 128 * 4 + 256];
  Sig sg{};
  STL_MARK(0);
  {
    const unsigned junk = (unsigned)(uintptr_t)(LDS_AS char*)smem + 128 * KB + 128 * 12;
    const int w = __builtin_amdgcn_readfirstlane(threadIdx.x >> 6), ob = blockIdx.x >> 3, lane = threadIdx.x & 63;
    if (w == 1) TOUCH_QKV1(ob, lane, junk);
    if (w == 3) TOUCH_FUSED(ob, lane, junk);
  }
  if (threadIdx.x < NCNT) a.cnt[(threadIdx.x * 256 + blockIdx.x) * CNT_STRIDE] = 0u;
  gemm_phase<CfgQ1, 0, 0>(a, (LDS_AS char*)smem, sg, [](auto, int, int) {});
}

constexpr int ATK = 256, ANS = 4, AKB = ATK * HP * 2, AVP = ATK * 16 + 64, ASTAGE = AKB + 3 * AVP + 64;
constexpr int ZOFF = 630 * 256 + 192;
static_assert(ASTAGE % 256 == 0 && (AKB + 3 * AVP) % 256 == ZOFF % 256 && ZOFF + 2048 <= LDS_ARR && ANS * ASTAGE + 64 + 4 * JBUF <= ZOFF, "");
struct AFr { bf16x8 k0, k1; };
template <class Pre>
__device__ __forceinline__ void attn_phase(const u16* QKV, u16* O, KPrep pa, char* ws, int kid, LDS_AS char* lds,
                                           Sig& sg, unsigned* cnt, int dep, int sig, Pre pre) {
  constexpr bool FUSED = true;
  TSDECL; TS(0);
  const unsigned lds0 = (unsigned)(uintptr_t)lds;
  int tid = threadIdx.x;
  if constexpr (FUSED) asm volatile("" : "+v"(tid));
  const int lane = tid & 63;
  const int wid = __builtin_amdgcn_readfirstlane(tid >> 6);
  const int qg = wid & 3, kh = wid >> 2;
  const int b = FUSED ? opaque_bid() : (int)blockIdx.x;
  const int h = (b & 7) + 8 * (b >> 6), qb = (b >> 3) & 7;
  const int r32 = lane & 31, hi = lane >> 5;
  const int q0 = qb * 256 + qg * 64 + r32;
  const u16* Qh = QKV + (size_t)h * S * HP;
  const u16* Kh = QKV + (size_t)(H + h) * S * HP;
  const u16* Vh = QKV + (size_t)(2 * H + h) * S * HP;

#define ZJ 3
  if (tid >= 384) *(LDS_AS u32x4*)(lds + ZOFF + (tid - 384) * 16) = u32x4{0u, 0u, 0u, 0u};
  if (dep >= 0) {
    if (wid == 0) wait_dep<WDD, WSD>(cnt + (dep * 256 + h * 8 + qb) * CNT_STRIDE, 34, dep);
    barrier_raw();
  }
  TS(2);
  LDS_AS char* jbuf = lds + ANS * ASTAGE + 64 + (wid & 3) * JBUF;
  {
    const char* qs = (const char*)Qh + qb * 4096 + (wid & 3) * 1024 + lane * 16;
    glds<FUSED>(qs + (wid >> 2) * (S * 16), lds0 + 3 * ASTAGE + wid * 1024);
    if (wid < 4) glds<FUSED>(qs + 2 * (S * 16), lds0 + 3 * ASTAGE + (8 + wid) * 1024);
  }
  const char* dsrc[3];
#pragma unroll
  for (int i = 0; i < 3; ++i) {
    const int p = (wid >> 2) + 2 * i;
    dsrc[i] = (p < 3 ? (const char*)Kh + p * (S * 16) : (const char*)Vh + (p - 3) * (S * 16)) + (wid & 3) * 1024 + lane * 16;
  }
  const char* ssrc[3];
#pragma unroll
  for (int i = 0; i < 3; ++i) {
    const int p = (wid >> 2) + 2 * i;
    ssrc[i] = (p < 3 ? (const char*)Kh + p * (S * 16) : (const char*)Vh + (p - 3) * (S * 16)) + (wid & 3) * 1024;
  }
  const unsigned l16 = lane * 16;
  auto issue = [&](int t) {
    const unsigned sb = lds0 + (t % ANS) * ASTAGE + wid * 1024;
    unsigned sv;
    asm volatile("s_mov_b32 %0, m0\n\ts_mov_b32 m0, %5\n\ts_nop 0\n\tglobal_load_lds_dwordx4 %1, %2 sc1\n\t"
                 "s_add_u32 m0, %5, 0x2000\n\ts_nop 0\n\tglobal_load_lds_dwordx4 %1, %3 sc1\n\t"
                 "s_add_u32 m0, %5, %6\n\ts_nop 0\n\tglobal_load_lds_dwordx4 %1, %4 sc1\n\ts_mov_b32 m0, %0"
                 : "=&s"(sv) : "v"(l16), "s"(ssrc[0] + t * 4096), "s"(ssrc[1] + t * 4096), "s"(ssrc[2] + t * 4096), "s"(sb),
                   "s"(16384 + 64 + (wid >> 2) * 64) : "memory", "scc");
  };
  issue(0);
  if (AIS < 2) issue(1);
  if (AIS < 1) issue(2);
  const int jsh = (wid & 3) * 8;
  const bool hasjob = dep < 0 && wid < 4 && b <= (int)((0x9FDF8FFFu >> jsh) & 255u);
  const int job = hasjob ? b + 16 * (int)((0x27191505u >> jsh) & 255u) - (wid == 1 && b >= 64 ? 400 : 0) : NJOB;

  const int koff = r32 * 16 + hi * 4096;
  const int k1off = r32 * 16 + 8192 + hi * 8;
  const int voff = AKB + (4 * hi + ((lane & 15) >> 2)) * 16 + (((lane >> 4) & 1) * 2 + ((lane >> 1) & 1)) * AVP + (lane & 1) * 8;
  const int khoff = kh * (128 * 16);

  JobL jl{};
  if (hasjob) jobs_prep(jl, pa, job, lane, kh);
  LDS_AS char* jbl = jbuf;
  waitv<6 - 3 * AIS>();
  if (AIS >= 2) issue(1);
  if (AIS >= 1) issue(2);
  barrier_raw();
  TS(1);
  if (hasjob) __builtin_amdgcn_s_setprio(3);
  if (hasjob) jobs_load<0>(jl, jbl, lane);
  typedef bf16x4 k1_t;
#define MFQ1(k, q, c) __builtin_amdgcn_mfma_f32_32x32x8bf16_1k(k, q, c, 0, 0, 0)
  bf16x8 qa0, qb0;
  k1_t qa1, qb1;
  {
    const LDS_AS char* qp = lds + 3 * ASTAGE + (qg * 64 + r32) * 16;
    qa0 = *(const LDS_AS bf16x8*)(qp + hi * 4096);
    qb0 = *(const LDS_AS bf16x8*)(qp + 512 + hi * 4096);
    qa1 = *(const LDS_AS k1_t*)(qp + 8192 + hi * 8);
    qb1 = *(const LDS_AS k1_t*)(qp + 512 + 8192 + hi * 8);
  }
  {
    bf16x8 kf0 = *(const LDS_AS bf16x8*)(lds + koff);
    k1_t kf1 = *(const LDS_AS k1_t*)(lds + k1off);
    f32x16 sa = __builtin_amdgcn_mfma_f32_32x32x16_bf16(kf0, qa0, f32x16{}, 0, 0, 0);
    f32x16 sb = __builtin_amdgcn_mfma_f32_32x32x16_bf16(kf0, qb0, f32x16{}, 0, 0, 0);
    sa = MFQ1(kf1, qa1, sa);
    sb = MFQ1(kf1, qb1, sb);
    float ma = sa[0], mb = sb[0];
#pragma unroll
    for (int r = 1; r < 16; ++r) { ma = fmaxf(ma, sa[r]); mb = fmaxf(mb, sb[r]); }
    ma = fmaxf(ma, __shfl_xor(ma, 32));
    mb = fmaxf(mb, __shfl_xor(mb, 32));
    const unsigned pk = pack2(-ma, -mb);
    u32x2 ta = __builtin_bit_cast(u32x2, qa1), tb = __builtin_bit_cast(u32x2, qb1);
    if (hi) { ta[0] = pk & 0xffffu; tb[0] = pk >> 16; }
    qa1 = __builtin_bit_cast(k1_t, ta);
    qb1 = __builtin_bit_cast(k1_t, tb);
  }

  f32x16 oa = {}, obb = {};
  auto tbase = [&](int i) -> const LDS_AS char* { return lds + ((i >> 2) % ANS) * ASTAGE + khoff + (i & 3) * (32 * 16); };
  const bool kz = false, vz = (ZJ & 2) && (lane & 18) == 18;
  unsigned kmul = kz ? 0u : 1u, vmul = vz ? 0u : 1u;
  asm volatile("" : "+v"(kmul), "+v"(vmul));
  const int k1o = kz ? ZOFF : khoff + k1off, vo = vz ? ZOFF + voff - (AKB + 3 * AVP) : khoff + voff;
  int ka = (int)lds0 + khoff + koff, k1a = (int)lds0 + k1o, va = (int)lds0 + vo;
  auto adv = [](int i) -> int { return ((i >> 2) % ANS) ? ASTAGE : -(ANS - 1) * ASTAGE; };
  auto rdK = [&](int i, bf16x8& k0, k1_t& k1, bool nx = false) __attribute__((always_inline)) {
    if (nx) {
      const int d = adv(i);
      ka += d;
      k1a += d;
      asm volatile("" : "+v"(ka), "+v"(k1a));
    }
    k0 = *(const LDS_AS bf16x8*)((const LDS_AS char*)(uintptr_t)ka + (i & 3) * (32 * 16));
    k1 = *(const LDS_AS k1_t*)((const LDS_AS char*)(uintptr_t)k1a + (i & 3) * (32 * 16));
  };
  auto rdV = [&](int i, bf16x8& v0, bf16x8& v1, bool nx = false) __attribute__((always_inline)) {
    if (nx) {
      va += __mul24(adv(i), (int)vmul);
      asm volatile("" : "+v"(va));
    }
    const LDS_AS char* p = (const LDS_AS char*)(uintptr_t)va + (i & 3) * (32 * 16);
    bf16x4 a0 = lds_tr(p), a1 = lds_tr(p + 128), a2 = lds_tr(p + 256), a3 = lds_tr(p + 384);
    v0 = bf16x8{a0[0], a0[1], a0[2], a0[3], a1[0], a1[1], a1[2], a1[3]};
    v1 = bf16x8{a2[0], a2[1], a2[2], a2[3], a3[0], a3[1], a3[2], a3[3]};
  };
  auto expo = [&](const f32x16& s, int r0) -> bf16x8 {
    float p[8];
#pragma unroll
    for (int r = 0; r < 8; ++r) p[r] = __builtin_amdgcn_exp2f(s[r0 + r]);
    u32x4 pb = {pack2(p[0], p[1]), pack2(p[2], p[3]), pack2(p[4], p[5]), pack2(p[6], p[7])};
    return __builtin_bit_cast(bf16x8, pb);
  };
  auto expq = [&](const f32x16& s, int r0, unsigned& w0, unsigned& w1) {
    const float p0 = __builtin_amdgcn_exp2f(s[r0]), p1 = __builtin_amdgcn_exp2f(s[r0 + 1]);
    const float p2 = __builtin_amdgcn_exp2f(s[r0 + 2]), p3 = __builtin_amdgcn_exp2f(s[r0 + 3]);
    w0 = pack2(p0, p1); w1 = pack2(p2, p3);
  };
  auto mk = [&](const unsigned* w) -> bf16x8 { return __builtin_bit_cast(bf16x8, u32x4{w[0], w[1], w[2], w[3]}); };
  bf16x8 kc0, kn0, vc0, vc1, vn0, vn1;
  k1_t kc1, kn1;
  bf16x8 pb2 = {0, 0, 0, 0, 0, 0, 0, 0}, vp1 = {0, 0, 0, 0, 0, 0, 0, 0};
  f32x16 sca, scb, sna, snb;
  float eb[4] = {0.f, 0.f, 0.f, 0.f};
  unsigned pbw[4] = {0u, 0u, 0u, 0u};
  rdK(0, kc0, kc1);
  rdK(1, kn0, kn1);
  rdV(0, vc0, vc1);
  sca = __builtin_amdgcn_mfma_f32_32x32x16_bf16(kc0, qa0, f32x16{}, 0, 0, 0);
  scb = __builtin_amdgcn_mfma_f32_32x32x16_bf16(kc0, qb0, f32x16{}, 0, 0, 0);
  sca = MFQ1(kc1, qa1, sca);
  scb = MFQ1(kc1, qb1, scb);
  kc0 = kn0; kc1 = kn1;
  constexpr int NTILE = S / ATK;
#define SB0 __builtin_amdgcn_sched_barrier(0)
  auto stg = [&](int) {};
  unsigned acnt = lds0 + LDS_ARR + (dep < 0 ? 48 : 52), aone = 1u, apk = 0u;
  asm volatile("" : "+v"(acnt), "+v"(aone));
  auto arrive = [&]() __attribute__((always_inline)) {
    asm volatile("s_mov_b64 exec, 1\n\tds_add_u32 %0, %1\n\ts_mov_b64 exec, -1" :: "v"(acnt), "v"(aone) : "memory");
  };
  auto peek = [&]() __attribute__((always_inline)) { asm volatile("ds_read_b32 %0, %1" : "=v"(apk) : "v"(acnt) : "memory"); };
  auto await = [&](int t) __attribute__((always_inline)) {
    unsigned x, n;
    asm volatile("s_waitcnt lgkmcnt(0)\n\tv_readfirstlane_b32 %1, %0\n\ts_cmp_ge_u32 %1, %4\n\ts_cbranch_scc1 2f\n\ts_mov_b32 %2, 0x100000\n"
                 "1:\n\tds_read_b32 %0, %3\n\ts_sub_u32 %2, %2, 1\n\ts_cbranch_scc1 2f\n\ts_waitcnt lgkmcnt(0)\n\tv_readfirstlane_b32 %1, %0\n\t"
                 "s_cmp_lt_u32 %1, %4\n\ts_cbranch_scc1 1b\n2:"
                 : "+v"(apk), "=&s"(x), "=&s"(n) : "v"(acnt), "s"(8 * t + 8) : "memory", "scc");
  };
  auto tile = [&](int t, auto iic) __attribute__((always_inline)) {
    constexpr int ii = decltype(iic)::value & 3;
    constexpr bool pk = decltype(iic)::value == 1;
    const int i = t * 4 + ii;
    unsigned pw[4];
#define PRM 1
    __builtin_amdgcn_s_setprio(ii == 2 ? 3 : ii == 3 ? 2 : ii == 0 ? 1 : 0);
    SB0;
    obb = __builtin_amdgcn_mfma_f32_32x32x16_bf16(vp1, pb2, obb, 0, 0, 0);
    rdK(i + 2, kn0, kn1, ii == 2);
    expq(sca, 0, pw[0], pw[1]);
    SB0;
    sna = __builtin_amdgcn_mfma_f32_32x32x16_bf16(kc0, qa0, f32x16{}, 0, 0, 0);
    rdV(i + 1, vn0, vn1, ii == 3);
    expq(sca, 4, pw[2], pw[3]);
    SB0;
    oa = __builtin_amdgcn_mfma_f32_32x32x16_bf16(vc0, mk(pw), oa, 0, 0, 0);
    expq(sca, 8, pw[0], pw[1]);
    SB0;
    sna = MFQ1(kc1, qa1, sna);
    expq(sca, 12, pw[2], pw[3]);
    SB0;
    oa = __builtin_amdgcn_mfma_f32_32x32x16_bf16(vc1, mk(pw), oa, 0, 0, 0);
    if constexpr (pk && PKP == 0) peek();
    expq(scb, 0, pw[0], pw[1]);
    SB0;
    snb = __builtin_amdgcn_mfma_f32_32x32x16_bf16(kc0, qb0, f32x16{}, 0, 0, 0);
    if constexpr (pk && PKP == 1) peek();
    expq(scb, 4, pw[2], pw[3]);
    SB0;
    obb = __builtin_amdgcn_mfma_f32_32x32x16_bf16(vc0, mk(pw), obb, 0, 0, 0);
    expq(scb, 8, pw[0], pw[1]);
    SB0;
    snb = MFQ1(kc1, qb1, snb);
    expq(scb, 12, pw[2], pw[3]);
    asm volatile("" : "+v"(pw[0]), "+v"(pw[1]), "+v"(pw[2]), "+v"(pw[3]));
    SB0;
    pb2 = mk(pw); vp1 = vc1;
    sca = sna; scb = snb; kc0 = kn0; kc1 = kn1; vc0 = vn0; vc1 = vn1;
  };
  auto stage = [&](int t, auto hook) __attribute__((always_inline)) {
    tile(t, IC<0>{}); tile(t, IC<1>{});
    __builtin_amdgcn_s_setprio(3);
    hook();
    tile(t, IC<2>{}); tile(t, IC<3>{});
  };
  static_assert(NTILE == 8, "");
  if (hasjob) waitv<3 + 4>(); else waitv<3>();
  arrive();
  stg(0);
  stage(0, [&]() __attribute__((always_inline)) {
    await(0);
    stg(8);
    __builtin_amdgcn_s_setprio(3);
    issue(3);
    if (hasjob) jobs_load<1>(jl, jbl, lane);
  });
  if (hasjob) __builtin_amdgcn_s_setprio(3);
  if (hasjob) jobs_load<2>(jl, jbuf, lane);
  const bool chk = dep < 0 && wid == 7;
#define TC2 0
  const bool tch = wid == 6 && ((TC2 >> (dep < 0 ? 0 : 1)) & 1);
  const int tsp = hasjob ? 1 : 8, xmask = 32 | (hasjob ? 30 : 0) | (tch ? 16 : 0);
  const int m15 = hasjob ? (((job >= 80 && job < 400) || job >= 624) ? 62 : 38) : 0, m8 = hasjob ? 24 : 0;
  int jcnt = 0;
  auto wcnt = [&](unsigned& tgt) __attribute__((always_inline)) -> const unsigned* {
    int l8 = (lane & 3) * 8, bb = b;
    asm volatile("" : "+v"(l8), "+s"(bb));
    const int cbr = (bb & 1) * 4 + ((bb >> 3) & 3);
    const unsigned ids = cbr | (8 + (bb & 3) * 4 + ((bb >> 3) & 3)) << 8 | (24 + cbr) << 16 | (32 + (bb & 1) * 8 + ((bb >> 3) & 7)) << 24;
    tgt = ((10u | 20u << 8 | 28u << 16 | 10u << 24) >> l8) & 255u;
    return cnt + (8 * 256 + ((ids >> l8) & 255u)) * CNT_STRIDE;
  };
#pragma nounroll
  for (int t = 1; t < 6; ++t) {
    asm volatile("s_waitcnt vmcnt(15)\n\ts_bitcmp1_b32 %1, %0\n\ts_cbranch_scc1 1f\n\ts_waitcnt vmcnt(8)\n\ts_bitcmp1_b32 %2, %0\n\t"
                 "s_cbranch_scc1 1f\n\ts_waitcnt vmcnt(3)\n1:" :: "s"(t), "s"(m15), "s"(m8) : "scc", "memory");
    arrive();
    stg(t);
    stage(t, [&]() __attribute__((always_inline)) {
      await(t);
      stg(8 + t);
      if (__builtin_expect((xmask >> t) & 1, 0)) {
        __builtin_amdgcn_s_setprio(3);
        if (t == 5) {
          if (dep >= 0) {
            pre(IC<0>{}, wid, lane);
          } else if (chk) {
            unsigned wtgt;
            const unsigned* wptr = wcnt(wtgt);
            glds4c(wptr, lds0 + ANS * ASTAGE + 64);
          }
          return;
        }
        if (t == 4) {
          if (tch) {
            TOUCH_FUSED(b >> 3, lane, lds0 + ANS * ASTAGE + 64 + 256);
          } else {
            waitv<0>();
            if (lane == 0)
              __hip_atomic_fetch_add((gu32*)(cnt + (8 * 256 + jcnt) * CNT_STRIDE), 1u, __ATOMIC_RELAXED, __HIP_MEMORY_SCOPE_AGENT);
          }
        } else if (t == 1) {
          jobs_load<3>(jl, jbuf, lane);
        } else {
          if (t == 2) waitv<3>();
          jcnt = jobs_part(ws, job, t - 2, jbuf, lds + ZOFF, lane);
        }
      }
      issue(t + 3);
    });
  }
  auto check6 = [&]() __attribute__((always_inline)) {
    if (dep >= 0) waitv<4>(); else waitv<0>();
    if (__builtin_expect(chk, 0)) {
      unsigned wtgt, spins = 0;
      const unsigned* wptr = wcnt(wtgt);
      unsigned wrdy = *(const LDS_AS unsigned*)(lds + ANS * ASTAGE + 64 + lane * 4);
#pragma nounroll
      while (__any(wrdy < wtgt)) {
        __builtin_amdgcn_s_sleep(1);
        asm volatile("global_load_dword %0, %1, off sc1\n\ts_waitcnt vmcnt(0)" : "=v"(wrdy) : "v"(wptr) : "memory");
        if (++spins > 400000u) break;
      }
    }
  };
  check6();
  arrive();
  stg(6);
  stage(6, [&]() __attribute__((always_inline)) {
    await(6);
    stg(14);
    __builtin_amdgcn_s_setprio(3);
    if (dep < 0) pre(IC<0>{}, wid, lane);
    pre(IC<1>{}, wid, lane);
  });
  TS(3);
  stg(7);
  tile(7, IC<0>{}); tile(7, IC<5>{}); tile(7, IC<2>{}); tile(7, IC<3>{});
  stg(15);
  obb = __builtin_amdgcn_mfma_f32_32x32x16_bf16(vp1, pb2, obb, 0, 0, 0);
  TS(4);
  f32x4 o4[3];
  {
    LDS_AS f32x4* ex = (LDS_AS f32x4*)(lds + ANS * ASTAGE + 64);
#pragma unroll
    for (int r = 0; r < 3; ++r) {
      const f32x4 ta = {oa[4 * r], oa[4 * r + 1], oa[4 * r + 2], oa[4 * r + 3]};
      const f32x4 tb = {obb[4 * r], obb[4 * r + 1], obb[4 * r + 2], obb[4 * r + 3]};
      ex[(wid * 3 + r) * 64 + lane] = kh ? ta : tb;
      o4[r] = kh ? tb : ta;
    }
    barrier_raw();
    TS(5);
#pragma unroll
    for (int r = 0; r < 3; ++r) o4[r] += ex[((wid ^ 4) * 3 + r) * 64 + lane];
  }
  const int q = q0 + 32 * kh;
  __amdgpu_buffer_rsrc_t ro = __builtin_amdgcn_make_buffer_rsrc(O, 0, 0x7fffffff, 0x00020000);
  const unsigned oA = (unsigned)((2 * h + hi) * PS + q * 16), oC = (unsigned)(64 * PS + (h * S + q) * 8);
  const auto lsw = __builtin_amdgcn_permlane32_swap(__float_as_uint(o4[2][0]), __float_as_uint(o4[2][0]), false, false);
  const float ls = __uint_as_float(lsw[1]);
  bool bad = !(ls < 1.0e18f);
#pragma unroll
  for (int r = 0; r < 12; ++r) bad = bad || !(fabsf(o4[r >> 2][r & 3]) < 3.0e38f);
  if (__builtin_expect(__any(bad), 0)) {
    auto ldr = [&](const u16* base, int row, float* v) {
#pragma unroll
      for (int c = 0; c < 5; ++c) {
        const u32x2 t = ld8c(base, (unsigned)((c >> 1) * (S * 16) + row * 16 + (c & 1) * 8));
        v[4 * c] = lo2f(t[0]); v[4 * c + 1] = hi2f(t[0]); v[4 * c + 2] = lo2f(t[1]); v[4 * c + 3] = hi2f(t[1]);
      }
    };
    float qv[DH];
    ldr(Qh, q, qv);
    float m = -INFINITY, lsum = 0.f, ov[DH];
#pragma unroll
    for (int d = 0; d < DH; ++d) ov[d] = 0.f;
    for (int key = 0; key < S; ++key) {
      float kv[DH];
      ldr(Kh, key, kv);
      float sc = 0.f;
#pragma unroll
      for (int d = 0; d < DH; ++d) sc += qv[d] * kv[d];
      const float mn = fmaxf(m, sc);
      const float al = __builtin_amdgcn_exp2f(m - mn), pe = __builtin_amdgcn_exp2f(sc - mn);
      ldr(Vh, key, kv);
      lsum = lsum * al + pe;
#pragma unroll
      for (int d = 0; d < DH; ++d) ov[d] = ov[d] * al + pe * kv[d];
      m = mn;
    }
    const float rl = 1.f / lsum;
    u32x4 w;
#pragma unroll
    for (int e = 0; e < 4; ++e)
      w[e] = hi ? pack2(ov[8 + 2 * e] * rl, ov[9 + 2 * e] * rl) : pack2(ov[2 * e] * rl, ov[2 * e + 1] * rl);
    __builtin_amdgcn_raw_buffer_store_b128(w, ro, oA, 0, 16);
    if (!hi) __builtin_amdgcn_raw_buffer_store_b64(u32x2{pack2(ov[16] * rl, ov[17] * rl), pack2(ov[18] * rl, ov[19] * rl)}, ro, oC, 0, 16);
  } else {
    const float rl = __builtin_amdgcn_rcpf(ls);
    const auto s0 = __builtin_amdgcn_permlane32_swap(pack2(o4[0][0] * rl, o4[0][1] * rl), pack2(o4[1][0] * rl, o4[1][1] * rl), false, false);
    const auto s1 = __builtin_amdgcn_permlane32_swap(pack2(o4[0][2] * rl, o4[0][3] * rl), pack2(o4[1][2] * rl, o4[1][3] * rl), false, false);
    __builtin_amdgcn_raw_buffer_store_b128(u32x4{s0[0], s1[0], s0[1], s1[1]}, ro, oA, 0, 16);
    if (!hi) __builtin_amdgcn_raw_buffer_store_b64(u32x2{pack2(o4[2][0] * rl, o4[2][1] * rl), pack2(o4[2][2] * rl, o4[2][3] * rl)}, ro, oC, 0, 16);
  }
  if constexpr (FUSED) {
    __builtin_amdgcn_s_setprio(0);
    sg.p = lane < 32 ? cnt + (sig * 256 + qb * 32 + lane) * CNT_STRIDE : nullptr;
    sg.id = sig;
    sg.nw = 8;
  }
  TS(6);
  TSFLUSH(kid);
}

#define WO_P1 0
#define QKV_NS 4
#define GU_NS 3
#define RES_NS 4
struct FusedArgs {
  const u16* x0; u16* out;
  PrepArgs pa;
};
constexpr size_t A_ST = (size_t)S * NPART * 8, A_X = (size_t)PS * (D / 8), A_H = (size_t)PS * (DFP / 8);
constexpr size_t A_O = (size_t)64 * PS + (size_t)H * S * 8, A_QKV = (size_t)3 * H * S * HP * 2 + 4096;
constexpr size_t L_ST1 = 0, L_ST2 = L_ST1 + A_ST, L_X1 = L_ST2 + A_ST, L_O = L_X1 + A_X, L_QKV = L_O + A_O,
                 L_H = L_QKV + A_QKV, L_X2 = L_H + A_H;
static_assert(PS == S * 16 && A_H <= A_QKV, "");
extern "C" __global__ __launch_bounds__(512) void fused_kernel(FusedArgs fa) {
  __shared__ __attribute__((aligned(16))) char smem[LDS_TOTAL];
  LDS_AS char* lds = (LDS_AS char*)smem;
  const int layer = blockIdx.y;
  if (layer == 0) {
    if (__builtin_amdgcn_readfirstlane(threadIdx.x >> 6) == 1) TOUCH_FUSED(blockIdx.x >> 3, threadIdx.x & 63, (unsigned)(uintptr_t)lds + 90 * KB);
    if (threadIdx.x < 16) ((LDS_AS unsigned*)(lds + LDS_ARR))[threadIdx.x] = 0u;
  }
  Sig sg{nullptr, 0, 0};
  typedef const __attribute__((address_space(4))) FusedArgs* KArg;
  auto opq = [&]() __attribute__((always_inline)) { char* w = fa.pa.ws; asm volatile("" : "+s"(w)); return w; };
  auto kar = [&]() __attribute__((always_inline)) { KArg k = (KArg)__builtin_amdgcn_kernarg_segment_ptr(); asm volatile("" : "+s"(k)); return k; };
  {
    {
      char* ws = opq();
      char* act = ws + W_ACT;
      attn_phase((const u16*)(act + L_QKV + (1 - layer) * A_QKV), (u16*)(act + L_O), &kar()->pa, ws, layer * 5 + 1, lds, sg, (unsigned*)(ws + W_CNT),
                 layer ? 3 : -1, layer ? 4 : 7,
                 [&](auto sc, int wv, int lane) {
                   constexpr int s = decltype(sc)::value;
                   const u16* Po = (const u16*)(opq() + W_PO);
                   if constexpr (s == 0) prefetch_r<RWo, 8, 2, 3>(Po, lds, wv, lane);
                   prefetch_r<RWo, 8, s, s + 1>(Po, lds, wv, lane);
                 });
    }
    {
      char* ws = opq();
      char* act = ws + W_ACT;
      GemmArgs a{}; a.X = (const u16*)(act + L_O); a.W0 = (const u16*)(ws + W_PO); a.out0 = (u16*)(act + L_X1); a.stats_out = (float2*)(act + L_ST1);
      a.res = layer ? (const u16*)(act + L_X2) : fa.x0; a.rps = layer ? PS : 16; a.rrs = layer ? 16 : D * 2;
      a.ops = PS; a.ors = 16; a.wt = 1; a.kid = layer * 5 + 2; a.cnt = (unsigned*)(ws + W_CNT); a.dep = layer ? 4 : 7; a.sig = layer ? 5 : 0;
      res_phase<RWo>(a, lds, sg, [&](auto sc, int lw, int lane) {
        constexpr int s = decltype(sc)::value;
        char* w = opq();
        if constexpr (s == WOH) prefetch_w<CfgGU, 4, 0, 1>((const u16*)(w + W_PG), (const u16*)(w + W_PU), lds, lw, lane);
        if constexpr (s == 100) prefetch_w<CfgGU, 4, 1, 3>((const u16*)(w + W_PG), (const u16*)(w + W_PU), lds, lw, lane);
      });
    }
    {
      char* ws = opq();
      char* act = ws + W_ACT;
      GemmArgs a{}; a.X = (const u16*)(act + L_X1); a.W0 = (const u16*)(ws + W_PG); a.W1 = (const u16*)(ws + W_PU);
      a.stats_in = (const float2*)(act + L_ST1);
      a.cs0 = (const float*)(ws + W_CSG); a.out0 = (u16*)(act + L_H); a.kid = layer * 5 + 3; a.cnt = (unsigned*)(ws + W_CNT);
      a.dep = layer ? 5 : 0; a.sig = layer ? 6 : 1;
      gemm_phase<CfgGU, 0, 0>(a, lds, sg, [&](auto sc, int lw, int lane) {
        constexpr int s = decltype(sc)::value;
        prefetch_r<RDn, 4, (s == 0 ? 0 : 2), (s == 0 ? 2 : 5)>((const u16*)(opq() + W_PD), lds, lw, lane);
      });
    }
    {
      char* ws = opq();
      char* act = ws + W_ACT;
      GemmArgs a{}; a.X = (const u16*)(act + L_H); a.W0 = (const u16*)(ws + W_PD); a.res = (const u16*)(act + L_X1); a.rps = PS; a.rrs = 16;
      a.out0 = layer ? fa.out : (u16*)(act + L_X2); a.stats_out = (float2*)(act + L_ST2);
      a.ops = layer ? 16 : PS; a.ors = layer ? D * 2 : 16; a.wt = layer ? 0 : 1; a.kid = layer * 5 + 4; a.cnt = (unsigned*)(ws + W_CNT);
      a.dep = layer ? 6 : 1; a.sig = 2;
      res_phase<RDn>(a, lds, sg, [&](auto sc, int lw, int lane) {
        constexpr int s = decltype(sc)::value;
        if (s == 100 && layer) return;
        static_assert(DNH1 >= 9 && DNH0 >= 11, "");
        const u16* Pq = (const u16*)(opq() + W_PQ);
        if constexpr (s == DNH0) prefetch_w<CfgQ2, 4, 0, 1>(Pq, nullptr, lds, lw, lane);
        if constexpr (s == DNH1) prefetch_w<CfgQ2, 4, 1, 2>(Pq, nullptr, lds, lw, lane);
        if constexpr (s == 100) prefetch_w<CfgQ2, 4, 2, 4>(Pq, nullptr, lds, lw, lane);
      });
    }
    if (layer) { waitv<0>(); STL_MARK(1); return; }
    {
      char* ws = opq();
      char* act = ws + W_ACT;
      GemmArgs a{}; a.X = (const u16*)(act + L_X2); a.W0 = (const u16*)(ws + W_PQ); a.stats_in = (const float2*)(act + L_ST2);
      a.cs0 = (const float*)(ws + W_CSQ);
      a.out0 = (u16*)(act + L_QKV); a.kid = 5; a.cnt = (unsigned*)(ws + W_CNT); a.dep = 2; a.sig = 3;
      gemm_phase<CfgQ2, 0, 0>(a, lds, sg, [](auto, int, int) {});
    }
    if (sg.nw) { waitv<0>(); do_signal(sg, lds, threadIdx.x & 63); }
    asm volatile("s_waitcnt lgkmcnt(0)\n\ts_getpc_b64 s[4:5]\n\ts_add_u32 s4, s4, fused_kernel@rel32@lo+4\n\t"
                 "s_addc_u32 s5, s5, fused_kernel@rel32@hi+12\n\ts_mov_b64 exec, -1\n\ts_nop 4\n\ts_setpc_b64 s[4:5]"
                 :: "{v0}"(threadIdx.x), "{s[0:1]}"(__builtin_amdgcn_kernarg_segment_ptr()), "{s2}"(blockIdx.x), "{s3}"(1)
                 : "s4", "s5", "scc", "memory");
    __builtin_unreachable();
  }
}

extern "C" __device__ __attribute__((used, noinline)) void code_end_mark() { asm volatile("s_nop 0"); }

extern "C" void kernel_launch(void* const* d_in, const int* in_sizes, int n_in, void* d_out, int out_size,
                              void* d_ws, size_t ws_size, hipStream_t stream) {
  char* ws = (char*)d_ws;
  {
    GemmArgs a{}; a.X = (const u16*)d_in[0]; a.W0 = (const u16*)d_in[1]; a.out0 = (u16*)(ws + W_ACT + L_QKV + A_QKV); a.kid = 0;
    a.cnt = (unsigned*)(ws + W_CNT);
    qkv1_kernel<<<256, 512, 0, stream>>>(a);
  }
  FusedArgs f{};
  f.x0 = (const u16*)d_in[0]; f.out = (u16*)d_out;
  f.pa.W[0] = (const u16*)d_in[1]; f.pa.W[1] = (const u16*)d_in[4]; f.pa.W[2] = (const u16*)d_in[5];
  f.pa.W[3] = (const u16*)d_in[2]; f.pa.W[4] = (const u16*)d_in[6];
  f.pa.ws = ws;
  fused_kernel<<<256, 512, 0, stream>>>(f);
}
```
